```python
import torch

def _softmax(x, axis=-1):
    return torch.softmax(x, dim=axis)
def _gelu(x):
    return torch.nn.functional.gelu(x, approximate="tanh")
def _silu(x):
    return torch.nn.functional.silu(x)
def _sigmoid(x):
    return torch.sigmoid(x)
def _layernorm(x, axis=-1, eps=1e-5):
    m = x.mean(dim=axis, keepdim=True)
    v = x.var(dim=axis, keepdim=True, unbiased=False)
    return (x - m) / torch.sqrt(v + eps)
def _scatter_add(tgt, idx, src, axis=0):
    return tgt.clone().index_add_(axis, idx, src)
def _segment_sum(src, seg, n):
    out = torch.zeros((n,) + src.shape[1:], dtype=src.dtype)
    return out.index_add_(0, seg, src)
def _pad(x, pw):
    flat = tuple(v for pair in reversed(pw) for v in pair)
    return torch.nn.functional.pad(x, flat)

kernel_name = 'recurrent_scan__sliding_block_cached__mixer_token_mlp__decode'

def setup_inputs():

    _w = (lambda a: (lambda t: t.to(torch.bfloat16) if t.is_floating_point() else t)(torch.as_tensor(a)))
    def _A_setup():
        B, Sq, D = 1, 1, 512
        Hq, Hkv, Sk, Dh = 32, 8, 32768, 128
        assert Hq % Hkv == 0, (Hq, Hkv)
        return {
            "x": _w((lambda *s: torch.randn(*s, dtype=torch.bfloat16) * 0.4)(B, Sq, D)),
            "k_cache": _w((lambda *s: torch.randn(*s, dtype=torch.bfloat16) * 1.6)(B, Hkv, Sk, Dh)),
            "v_cache": _w((lambda *s: torch.randn(*s, dtype=torch.bfloat16) * 1.6)(B, Hkv, Sk, Dh)),
            "wq": _w((lambda *s: torch.randn(*s, dtype=torch.bfloat16) * 1.6)(D, Hq * Dh) * (D ** -0.5)),
            "wo": _w((lambda *s: torch.randn(*s, dtype=torch.bfloat16) * 1.6)(Hq * Dh, D) * ((Hq * Dh) ** -0.5)),
        }
    _A_in = _A_setup()

    def _B_setup():
        B, S, D, F = 1, 1, 512, 1792
        return {
            "x": _w((lambda *s: torch.randn(*s, dtype=torch.bfloat16) * 0.4)(B, S, D)),
            "w1": _w((lambda *s: torch.randn(*s, dtype=torch.bfloat16) * 1.6)(S, F) * (S ** -0.5)),
            "b1": _w((lambda *s: torch.randn(*s, dtype=torch.bfloat16) * 1.6)(F) * 0.02),
            "w2": _w((lambda *s: torch.randn(*s, dtype=torch.bfloat16) * 1.6)(F, S) * (F ** -0.5)),
            "b2": _w((lambda *s: torch.randn(*s, dtype=torch.bfloat16) * 1.6)(S) * 0.02),
        }
    _B_in = _B_setup()

    return {
        "A_x": _A_in["x"],
        "A_k_cache": _A_in["k_cache"],
        "A_v_cache": _A_in["v_cache"],
        "A_wq": _A_in["wq"],
        "A_wo": _A_in["wo"],
        "B_x": _B_in["x"],
        "B_w1": _B_in["w1"],
        "B_b1": _B_in["b1"],
        "B_w2": _B_in["w2"],
        "B_b2": _B_in["b2"],
    }

def reference(A_x, A_k_cache, A_v_cache, A_wq, A_wo, B_x, B_w1, B_b1, B_w2, B_b2):
    def _A_ref(x, k_cache, v_cache, wq, wo):
        B, Sq, D = x.shape
        Hq, Dh = 32, 128
        Hkv, Sk = k_cache.shape[1], k_cache.shape[2]
        W = min(4096, max(256, Sk // 4))
        rep = Hq // Hkv
        q = (lambda x, *d: x.permute(*d))((x @ wq).reshape(B, Sq, Hq, Dh), 0, 2, 1, 3)
        k = (lambda x, n, axis: torch.repeat_interleave(x, n, dim=axis))(k_cache[:, :, Sk - W:, :], rep, axis=1)
        v = (lambda x, n, axis: torch.repeat_interleave(x, n, dim=axis))(v_cache[:, :, Sk - W:, :], rep, axis=1)
        scores = torch.einsum("bhqd,bhkd->bhqk", q, k) * (Dh ** -0.5)
        p = _softmax(scores, axis=-1)
        o = torch.einsum("bhqk,bhkd->bhqd", p, v)
        return (lambda x, *d: x.permute(*d))(o, 0, 2, 1, 3).reshape(B, Sq, Hq * Dh) @ wo

    def _B_ref(x, w1, b1, w2, b2):
        B, S, D = x.shape
        h = _layernorm(x, axis=-1)
        u = torch.einsum("bsd,sf->bdf", h, w1) + b1
        u = _gelu(u)
        y = torch.einsum("bdf,fs->bds", u, w2) + b2
        return (lambda x, *d: x.permute(*d))(y, 0, 2, 1)

    x = A_x
    xn = _layernorm(x, axis=-1)
    s = _A_ref(xn, A_k_cache, A_v_cache, A_wq, A_wo)
    g = _silu(_B_ref(xn, B_w1, B_b1, B_w2, B_b2))
    return x + s * g
```

```cpp
#include <hip/hip_runtime.h>
#include <hip/hip_bf16.h>
#include <stdint.h>

typedef unsigned short u16;
typedef __attribute__((ext_vector_type(4))) unsigned u32x4;
__device__ __forceinline__ float bf2f(u16 v) { return __uint_as_float(((unsigned)v) << 16); }
__device__ __forceinline__ float rbf(float f) {
  unsigned u = __float_as_uint(f); u += 0x7FFF + ((u >> 16) & 1); return __uint_as_float(u & 0xFFFF0000u);
}
__device__ __forceinline__ float wsum(float v) { for (int m = 32; m >= 1; m >>= 1) v += __shfl_xor(v, m); return v; }
__device__ __forceinline__ float wmax(float v) { for (int m = 32; m >= 1; m >>= 1) v = fmaxf(v, __shfl_xor(v, m)); return v; }

constexpr int D = 512, HQ = 32, HKV = 8, SK = 32768, DH = 128, W = 4096, F = 1792;

__device__ __forceinline__ void ln8(float (&v)[8]) {
  float s = 0; for (int i = 0; i < 8; ++i) s += v[i];
  s = wsum(s); float mean = s * (1.f / 512.f);
  float q = 0; for (int i = 0; i < 8; ++i) { float d = v[i] - mean; q += d * d; }
  q = wsum(q); float var = rbf(q * (1.f / 512.f)); float mb = rbf(mean);
  float den = rbf(sqrtf(rbf(var + 1e-5f)));
  for (int i = 0; i < 8; ++i) v[i] = rbf(rbf(v[i] - mb) / den);
}

__global__ void k1(const u16* x, const u16* wq, const u16* w1, const u16* b1, const u16* w2, const u16* b2, float* q, float* g) {
  __shared__ float xn[512]; __shared__ float hh[512]; __shared__ float red[32][64];
  int tid = threadIdx.x, lane = tid & 63, wave = tid >> 6;
  if (wave == 0) {
    float v[8]; for (int i = 0; i < 8; ++i) v[i] = bf2f(x[lane * 8 + i]);
    ln8(v); for (int i = 0; i < 8; ++i) xn[lane * 8 + i] = v[i];
    ln8(v); for (int i = 0; i < 8; ++i) hh[lane * 8 + i] = v[i];
  }
  __syncthreads();
  int c0 = blockIdx.x * 64; int cg = tid & 7, rg = tid >> 3;
  float acc[8] = {0,0,0,0,0,0,0,0};
  for (int r = rg; r < 512; r += 32) {
    u32x4 w = *(const u32x4*)(wq + (size_t)r * 4096 + c0 + cg * 8);
    float xv = xn[r];
    for (int k = 0; k < 4; ++k) { acc[2*k] += xv * __uint_as_float(w[k] << 16); acc[2*k+1] += xv * __uint_as_float(w[k] & 0xffff0000u); }
  }
  for (int k = 0; k < 8; ++k) red[rg][cg * 8 + k] = acc[k];
  __syncthreads();
  if (tid < 64) { float s = 0; for (int r = 0; r < 32; ++r) s += red[r][tid]; q[c0 + tid] = rbf(s); }
  for (int dd = 0; dd < 2; ++dd) {
    int d = blockIdx.x * 8 + wave * 2 + dd; float h = hh[d]; float s = 0;
    for (int f = lane; f < F; f += 64) {
      float u = rbf(rbf(h * bf2f(w1[f])) + bf2f(b1[f]));
      float t = 0.7978845608028654f * (u + 0.044715f * u * u * u);
      float ge = rbf(0.5f * u * (1.f + tanhf(t)));
      s += ge * bf2f(w2[f]);
    }
    s = wsum(s); float y = rbf(rbf(s) + bf2f(b2[0]));
    if (lane == 0) g[d] = rbf(y / (1.f + __expf(-y)));
  }
}

__global__ void k2(const u16* kc, const u16* vc, const float* q, float* po, float* pml) {
  __shared__ float qs[512]; __shared__ float sc[4][128]; __shared__ float red[16][512];
  int tid = threadIdx.x, h = blockIdx.x & 7, s = blockIdx.x >> 3;
  qs[tid] = q[h * 512 + tid]; qs[tid + 256] = q[h * 512 + 256 + tid];
  __syncthreads();
  const u16* kb = kc + ((size_t)h * SK + (SK - W) + s * 128) * DH;
  const u16* vb = vc + ((size_t)h * SK + (SK - W) + s * 128) * DH;
  int c = tid & 15, r = tid >> 4;
  for (int kk = r; kk < 128; kk += 16) {
    u32x4 w = *(const u32x4*)(kb + kk * DH + c * 8);
    float a[4] = {0,0,0,0};
    for (int k = 0; k < 4; ++k) { float lo = __uint_as_float(w[k] << 16), hi = __uint_as_float(w[k] & 0xffff0000u);
      for (int g = 0; g < 4; ++g) a[g] += lo * qs[g * 128 + c * 8 + 2 * k] + hi * qs[g * 128 + c * 8 + 2 * k + 1]; }
    for (int g = 0; g < 4; ++g) { float v = a[g]; for (int m = 8; m >= 1; m >>= 1) v += __shfl_xor(v, m); if (c == 0) sc[g][kk] = rbf(rbf(v) * 0.08838834764831845f); }
  }
  __syncthreads();
  int wave = tid >> 6, lane = tid & 63;
  { float a = sc[wave][lane], b = sc[wave][lane + 64]; float m = wmax(fmaxf(a, b)); a = __expf(a - m); b = __expf(b - m); float l = wsum(a + b);
    sc[wave][lane] = a; sc[wave][lane + 64] = b; if (lane == 0) { pml[(blockIdx.x * 4 + wave) * 2] = m; pml[(blockIdx.x * 4 + wave) * 2 + 1] = l; } }
  __syncthreads();
  float acc[4][8]; for (int g = 0; g < 4; ++g) for (int k = 0; k < 8; ++k) acc[g][k] = 0;
  for (int kk = r; kk < 128; kk += 16) {
    u32x4 w = *(const u32x4*)(vb + kk * DH + c * 8);
    for (int g = 0; g < 4; ++g) { float p = sc[g][kk];
      for (int k = 0; k < 4; ++k) { acc[g][2*k] += p * __uint_as_float(w[k] << 16); acc[g][2*k+1] += p * __uint_as_float(w[k] & 0xffff0000u); } }
  }
  for (int g = 0; g < 4; ++g) for (int k = 0; k < 8; ++k) red[r][g * 128 + c * 8 + k] = acc[g][k];
  __syncthreads();
  for (int i = tid; i < 512; i += 256) { float t = 0; for (int rr = 0; rr < 16; ++rr) t += red[rr][i]; po[(size_t)blockIdx.x * 512 + i] = t; }
}

__global__ void k3(const float* po, const float* pml, const u16* wo, float* part) {
  __shared__ float os[16];
  int tid = threadIdx.x, h = blockIdx.x & 7, j = blockIdx.x >> 3; int g = j >> 3;
  if (tid < 16) {
    float M = -INFINITY; for (int s = 0; s < 32; ++s) M = fmaxf(M, pml[((s * 8 + h) * 4 + g) * 2]);
    float L = 0, o = 0;
    for (int s = 0; s < 32; ++s) { float w = __expf(pml[((s * 8 + h) * 4 + g) * 2] - M); L += w * pml[((s * 8 + h) * 4 + g) * 2 + 1]; o += w * po[(size_t)(s * 8 + h) * 512 + j * 16 + tid]; }
    os[tid] = rbf(o / L);
  }
  __syncthreads();
  for (int c = tid; c < 512; c += 256) { float a = 0; for (int r = 0; r < 16; ++r) a += os[r] * bf2f(wo[(size_t)(h * 512 + j * 16 + r) * 512 + c]); part[(size_t)blockIdx.x * 512 + c] = a; }
}
__global__ void k4(const float* part, const float* g, const u16* x, u16* out) {
  int c = blockIdx.x * 64 + (threadIdx.x & 63); int w = threadIdx.x >> 6; __shared__ float red[4][64];
  float a = 0; for (int b = w; b < 256; b += 4) a += part[(size_t)b * 512 + c];
  red[w][threadIdx.x & 63] = a; __syncthreads();
  if (w == 0) { float s = rbf(red[0][threadIdx.x] + red[1][threadIdx.x] + red[2][threadIdx.x] + red[3][threadIdx.x]);
    float o = rbf(bf2f(x[c]) + rbf(s * g[c])); out[c] = (u16)(__float_as_uint(o) >> 16); }
}

extern "C" void kernel_launch(void* const* d_in, const int* in_sizes, int n_in, void* d_out, int out_size, void* d_ws, size_t ws_size, hipStream_t stream) {
  const u16* x = (const u16*)d_in[0]; const u16* kc = (const u16*)d_in[1]; const u16* vc = (const u16*)d_in[2];
  const u16* wq = (const u16*)d_in[3]; const u16* wo = (const u16*)d_in[4];
  const u16* w1 = (const u16*)d_in[6]; const u16* b1 = (const u16*)d_in[7]; const u16* w2 = (const u16*)d_in[8]; const u16* b2 = (const u16*)d_in[9];
  float* ws = (float*)d_ws; float* q = ws; float* g = ws + 4096; float* pml = ws + 8192; float* po = ws + 16384; float* part = po + 256 * 512;
  k1<<<64, 256, 0, stream>>>(x, wq, w1, b1, w2, b2, q, g);
  k2<<<256, 256, 0, stream>>>(kc, vc, q, po, pml);
  k3<<<256, 256, 0, stream>>>(po, pml, wo, part);
  k4<<<8, 256, 0, stream>>>(part, g, x, (u16*)d_out);
}
```

```cpp
#include <hip/hip_runtime.h>
#include <hip/hip_bf16.h>
#include <stdint.h>

typedef unsigned short u16;
typedef unsigned long long u64;
typedef __attribute__((ext_vector_type(2))) unsigned u32x2;
typedef __attribute__((ext_vector_type(4))) unsigned u32x4;
typedef __attribute__((ext_vector_type(2))) float f32x2;
typedef __attribute__((ext_vector_type(4))) float f32x4;
typedef __attribute__((ext_vector_type(2))) __bf16 bf16x2;
typedef __attribute__((ext_vector_type(4))) __bf16 bf16x4;
typedef __attribute__((ext_vector_type(8))) __bf16 bf16x8;
#define LAS __attribute__((address_space(3)))
#define GAS __attribute__((address_space(1)))

constexpr int SK = 32768, WIN = 4096;
constexpr int L_WQ = 0;
constexpr int L_K = 16384;
constexpr int L_V = 49152;
constexpr int L_QP = 81920;
constexpr int L_QS = L_QP + 1024;
constexpr int L_P = L_QS + 1280;
constexpr int L_OA = L_P + 1280;
constexpr int L_H = L_OA + 4352;
constexpr int L_WM = L_H + 2048;
constexpr int L_GP = L_WM + 128;
constexpr int L_CT = L_GP + 64;
constexpr int L_END = L_CT + 16;
constexpr int W_EP = 0;
constexpr int W_Q = 4096;
constexpr int W_O = W_Q + 65536;
constexpr int W_F = W_O + 8 * 4 * 32 * 640;
constexpr int W_END = W_F + 131072;
constexpr int W_ST = 1 << 20;
constexpr unsigned SPIN_CAP = 1u << 16;
constexpr float L2E = 1.4426950408889634f;

#define STAMP(i) do {} while (0)

__device__ __forceinline__ unsigned pk(float a, float b) { f32x2 v = {a, b}; return __builtin_bit_cast(unsigned, __builtin_convertvector(v, bf16x2)); }
__device__ __forceinline__ float lo16(unsigned u) { return __uint_as_float(u << 16); }
__device__ __forceinline__ float hi16(unsigned u) { return __uint_as_float(u & 0xffff0000u); }
__device__ __forceinline__ float rbf(float a) { return lo16(pk(a, a)); }
template <int CTRL> __device__ __forceinline__ float dpp(float x) {
  return __builtin_bit_cast(float, __builtin_amdgcn_mov_dpp(__builtin_bit_cast(int, x), CTRL, 0xf, 0xf, true));
}
constexpr int XOR1 = 0xB1, XOR2 = 0x4E, HMIR = 0x141, ROR8 = 0x128;
__device__ __forceinline__ float sum16(float x) { x += dpp<XOR1>(x); x += dpp<XOR2>(x); x += dpp<HMIR>(x); x += dpp<ROR8>(x); return x; }
__device__ __forceinline__ float max16(float x) { x = fmaxf(x, dpp<XOR1>(x)); x = fmaxf(x, dpp<XOR2>(x)); x = fmaxf(x, dpp<HMIR>(x)); x = fmaxf(x, dpp<ROR8>(x)); return x; }
__device__ __forceinline__ float sw16sum(float x) { auto s = __builtin_amdgcn_permlane16_swap(__float_as_uint(x), __float_as_uint(x), false, false); return __uint_as_float(s[0]) + __uint_as_float(s[1]); }
__device__ __forceinline__ float sw32sum(float x) { auto s = __builtin_amdgcn_permlane32_swap(__float_as_uint(x), __float_as_uint(x), false, false); return __uint_as_float(s[0]) + __uint_as_float(s[1]); }
__device__ __forceinline__ float sw16max(float x) { auto s = __builtin_amdgcn_permlane16_swap(__float_as_uint(x), __float_as_uint(x), false, false); return fmaxf(__uint_as_float(s[0]), __uint_as_float(s[1])); }
__device__ __forceinline__ float sw32max(float x) { auto s = __builtin_amdgcn_permlane32_swap(__float_as_uint(x), __float_as_uint(x), false, false); return fmaxf(__uint_as_float(s[0]), __uint_as_float(s[1])); }
__device__ __forceinline__ float sum64(float x) { return sw32sum(sw16sum(sum16(x))); }

#define BAR() asm volatile("s_waitcnt lgkmcnt(0)\n\ts_barrier" ::: "memory")
#define VMWAIT(n) asm volatile("s_waitcnt vmcnt(" #n ")" ::: "memory")
#define DMA1(voff, sbase, ldsb) asm volatile("s_mov_b32 m0, %2\n\ts_nop 0\n\tglobal_load_lds_dwordx4 %0, %1 nt" :: "v"(voff), "s"(sbase), "s"(ldsb) : "memory")
#define DMA1O(voff, sbase, ldsb, OFF) asm volatile("s_mov_b32 m0, %2\n\ts_nop 0\n\tglobal_load_lds_dwordx4 %0, %1 offset:" #OFF " nt" :: "v"(voff), "s"(sbase), "s"(ldsb) : "memory")

template <typename T> __device__ __forceinline__ T lds_ld(unsigned a) { return *(LAS T*)(size_t)a; }
template <typename T> __device__ __forceinline__ void lds_st(unsigned a, T v) { *(LAS T*)(size_t)a = v; }
__device__ __forceinline__ bf16x8 trfrag(unsigned a0, unsigned a1) {
  bf16x4 a = __builtin_amdgcn_ds_read_tr16_b64_v4bf16((LAS bf16x4*)(size_t)a0);
  bf16x4 c = __builtin_amdgcn_ds_read_tr16_b64_v4bf16((LAS bf16x4*)(size_t)a1);
  return __builtin_shufflevector(a, c, 0, 1, 2, 3, 4, 5, 6, 7);
}
__device__ __forceinline__ f32x4 mfma(bf16x8 a, bf16x8 b, f32x4 c) { return __builtin_amdgcn_mfma_f32_16x16x32_bf16(a, b, c, 0, 0, 0); }
__device__ __forceinline__ unsigned tag8(float v, unsigned t8) { return (__float_as_uint(v) & 0xffffff00u) | t8; }
__device__ __forceinline__ unsigned bad4(u32x4 v, unsigned t8) { return ((v[0] ^ t8) | (v[1] ^ t8) | (v[2] ^ t8) | (v[3] ^ t8)); }

__device__ __forceinline__ void ln_stats(const float (&v)[8], float& mb, float& den) {
  float s = ((v[0] + v[1]) + (v[2] + v[3])) + ((v[4] + v[5]) + (v[6] + v[7]));
  s = sum64(s); const float mean = s * (1.f / 512.f);
  float q = 0.f;
#pragma unroll
  for (int i = 0; i < 8; ++i) { const float d = v[i] - mean; q = fmaf(d, d, q); }
  q = sum64(q);
  mb = rbf(mean);
  den = rbf(__builtin_sqrtf(rbf(rbf(q * (1.f / 512.f)) + 1e-5f)));
}
__device__ __forceinline__ float ln_apply(float x, float mb, float den) { return rbf(x - mb) / den; }

__global__ void __launch_bounds__(768) fused(const u16* __restrict__ x, const u16* __restrict__ kc, const u16* __restrict__ vc,
                                             const u16* __restrict__ wq, const u16* __restrict__ wo, const u16* __restrict__ w1,
                                             const u16* __restrict__ b1, const u16* __restrict__ w2, const u16* __restrict__ b2,
                                             u16* __restrict__ out, char* ws) {
  extern __shared__ __attribute__((aligned(16))) char smem[];
  const int b = blockIdx.x, h = b & 7, j = b >> 3;
  const int lane = threadIdx.x & 63, wave = __builtin_amdgcn_readfirstlane(threadIdx.x >> 6);
  const int G = lane >> 4, c = lane & 15;
  const __amdgpu_buffer_rsrc_t rs = __builtin_amdgcn_make_buffer_rsrc(ws, 0, W_END, 0x00020000);
  STAMP(0);
  if (wave < 4) {
    __builtin_amdgcn_s_setprio(3);
    const int ks = j & 3, cblk = j >> 2;
    const u32x4 xv = *(const GAS u32x4*)(x + lane * 8);
    const u32x4 xf = *(const GAS u32x4*)(x + ks * 128 + wave * 32 + G * 8);
    {
      const char* src = (const char*)wq + ((size_t)(ks * 128 + wave * 32) * 4096 + h * 512 + cblk * 64) * 2;
      const int r = lane >> 3, pos = lane & 7;
      const unsigned v0 = r * 8192 + ((((pos >> 1) ^ ((r >> 1) & 1)) << 1 | (pos & 1)) << 4);
      const unsigned v1 = v0 ^ 64;
      const unsigned l0 = L_WQ + wave * 4096;
      DMA1(v0, src, l0); DMA1(v1, src + 65536, l0 + 1024); DMA1(v0, src + 131072, l0 + 2048); DMA1(v1, src + 196608, l0 + 3072);
    }
    unsigned e32 = 0, xo = 0;
    if (wave == 0) {
      e32 = __hip_atomic_load((GAS unsigned*)(ws + W_EP), __ATOMIC_RELAXED, __HIP_MEMORY_SCOPE_AGENT);
      xo = *(const GAS unsigned*)(x + 64 * h + 2 * j);
      if (lane == 0) lds_st<unsigned>(L_CT, 0u);
    }
    BAR();
    STAMP(1);
    float v[8];
#pragma unroll
    for (int i = 0; i < 4; ++i) { v[2 * i] = lo16(xv[i]); v[2 * i + 1] = hi16(xv[i]); }
    float mb, den; ln_stats(v, mb, den);
    u32x4 af;
#pragma unroll
    for (int i = 0; i < 4; ++i) af[i] = pk(ln_apply(lo16(xf[i]), mb, den), ln_apply(hi16(xf[i]), mb, den));
    const unsigned t32 = e32 + 0x9E3779B1u;
    if (wave == 0 && lane == 0) lds_st<unsigned>(L_CT + 4, t32);
    STAMP(2);
    VMWAIT(0);
    STAMP(3);
    f32x4 acc[4];
    {
      const int q = c >> 2, p = c & 3, row = 8 * G + q, s = ((q >> 1) & 1) | ((G & 1) << 1);
      const unsigned base = L_WQ + wave * 4096 + row * 128 + (p >> 1) * 16 + (p & 1) * 8;
#pragma unroll
      for (int t = 0; t < 4; ++t) { const unsigned a = base + ((t ^ s) << 5);
        acc[t] = mfma(__builtin_bit_cast(bf16x8, af), trfrag(a, a + 512), f32x4{0.f, 0.f, 0.f, 0.f}); }
    }
    const float qp = G == 0 ? acc[0][0] : G == 1 ? acc[1][0] : G == 2 ? acc[2][0] : acc[3][0];
    lds_st<float>(L_QP + wave * 256 + lane * 4, qp);
    asm volatile("s_waitcnt lgkmcnt(0)" ::: "memory");
    unsigned old = 0;
    if (lane == 0) old = __hip_atomic_fetch_add((LAS unsigned*)(size_t)L_CT, 1u, __ATOMIC_RELAXED, __HIP_MEMORY_SCOPE_WORKGROUP);
    old = __builtin_amdgcn_readfirstlane(old);
    asm volatile("" ::: "memory");
    if (old == 3) {
      const unsigned t8 = lds_ld<unsigned>(L_CT + 4) & 0xffu;
      const unsigned a = L_QP + c * 16;
      const f32x4 s = (lds_ld<f32x4>(a) + lds_ld<f32x4>(a + 256)) + (lds_ld<f32x4>(a + 512) + lds_ld<f32x4>(a + 768));
      const u32x4 o = {tag8(s[0], t8), tag8(s[1], t8), tag8(s[2], t8), tag8(s[3], t8)};
      const unsigned off = lane < 16 ? (unsigned)(W_Q + ((h * 4 + ks) * 512 + cblk * 64) * 4 + c * 16) : 0x80000000u;
      __builtin_amdgcn_raw_buffer_store_b128(o, rs, off, 0, 16);
      STAMP(4);
    }
    if (wave == 1) {
      float u[8];
#pragma unroll
      for (int i = 0; i < 8; ++i) u[i] = rbf(ln_apply(v[i], mb, den));
      float mb2, den2; ln_stats(u, mb2, den2);
      f32x4 h0, h1;
#pragma unroll
      for (int i = 0; i < 4; ++i) { h0[i] = rbf(ln_apply(u[i], mb2, den2)); h1[i] = rbf(ln_apply(u[4 + i], mb2, den2)); }
      lds_st<f32x4>(L_H + lane * 32, h0); lds_st<f32x4>(L_H + lane * 32 + 16, h1);
    }
    if (wave != 0) return;
    const unsigned t8 = t32 & 0xffu;
    {
      const unsigned off = W_Q + h * 8192 + lane * 32;
      u32x4 g[8];
      for (unsigned spins = 0; spins < SPIN_CAP; ++spins) {
        asm volatile("" ::: "memory");
#pragma unroll
        for (int k = 0; k < 4; ++k) { g[2 * k] = __builtin_amdgcn_raw_buffer_load_b128(rs, off + k * 2048, 0, 16); g[2 * k + 1] = __builtin_amdgcn_raw_buffer_load_b128(rs, off + k * 2048 + 16, 0, 16); }
        unsigned bad = 0;
#pragma unroll
        for (int k = 0; k < 8; ++k) bad |= bad4(g[k], t8);
        if (__all((bad & 0xffu) == 0)) break;
      }
      float q[8];
#pragma unroll
      for (int e = 0; e < 8; ++e) q[e] = (__uint_as_float(g[e >> 2][e & 3]) + __uint_as_float(g[2 + (e >> 2)][e & 3])) + (__uint_as_float(g[4 + (e >> 2)][e & 3]) + __uint_as_float(g[6 + (e >> 2)][e & 3]));
      lds_st<u32x4>(L_QS + G * 320 + c * 16, u32x4{pk(q[0], q[1]), pk(q[2], q[3]), pk(q[4], q[5]), pk(q[6], q[7])});
    }
    STAMP(5);
    BAR();
    BAR();
    const f32x4 m0 = lds_ld<f32x4>(L_WM + (c & 3) * 32), m1 = lds_ld<f32x4>(L_WM + (c & 3) * 32 + 16);
    const float m = fmaxf(fmaxf(fmaxf(m0[0], m0[1]), fmaxf(m0[2], m0[3])), fmaxf(fmaxf(m1[0], m1[1]), fmaxf(m1[2], m1[3])));
    BAR();
    {
      f32x4 l = {0.f, 0.f, 0.f, 0.f};
      const u32x4 ones = {0x3f803f80u, 0x3f803f80u, 0x3f803f80u, 0x3f803f80u};
#pragma unroll
      for (int t = 0; t < 4; ++t) l = mfma(__builtin_bit_cast(bf16x8, ones), lds_ld<bf16x8>(L_P + (c & 3) * 320 + t * 64 + G * 16), l);
      const unsigned off = lane < 4 ? (unsigned)(W_O + ((h * 4 + lane) * 32 + j) * 640 + 512) : 0x80000000u;
      __builtin_amdgcn_raw_buffer_store_b128(u32x4{__float_as_uint(m), t32, __float_as_uint(l[0]), t32}, rs, off, 0, 16);
    }
    STAMP(6);
    BAR();
    const f32x4 g0 = lds_ld<f32x4>(L_GP + (lane >> 5) * 32), g1 = lds_ld<f32x4>(L_GP + (lane >> 5) * 32 + 16);
    const float ys = ((g0[0] + g0[1]) + (g0[2] + g0[3])) + ((g1[0] + g1[1]) + g1[2]);
    const float y = rbf(rbf(ys) + lo16(*(const GAS u16*)b2));
    const float gate = rbf(y * __builtin_amdgcn_rcpf(1.f + __builtin_amdgcn_exp2f(-L2E * y)));
    u32x2 f;
    {
      const unsigned off = W_F + ((h * 32 + (lane & 31)) * 64 + 2 * j + (lane >> 5)) * 8;
      for (unsigned spins = 0; spins < SPIN_CAP; ++spins) {
        asm volatile("" ::: "memory");
        f = __builtin_amdgcn_raw_buffer_load_b64(rs, off, 0, 16);
        if (__all(f[1] == t32)) break;
      }
    }
    STAMP(7);
    const float s = rbf(sw16sum(sum16(__uint_as_float(f[0]))));
    const float xs = lane < 32 ? lo16(xo) : hi16(xo);
    const float o = xs + rbf(s * gate);
    if ((lane & 31) == 0) out[64 * h + 2 * j + (lane >> 5)] = (u16)(pk(o, o) & 0xffffu);
    if (b == 0 && lane == 0) __hip_atomic_store((GAS unsigned*)(ws + W_EP), t32, __ATOMIC_RELAXED, __HIP_MEMORY_SCOPE_AGENT);
    STAMP(8);
    return;
  }
  const int w = wave - 4;
  const int g2 = j >> 3, cb = j & 7;
  u32x2 gw1, gb1, gw2;
  {
    const size_t rowoff = ((size_t)h * SK + (SK - WIN) + j * 128 + w * 16) * 256;
    const char* ksrc = (const char*)kc + rowoff; const char* vsrc = (const char*)vc + rowoff;
    const unsigned k0 = G * 256 + ((c ^ G) << 4);
    const unsigned va = G * 256 + ((c ^ (G << 1)) << 4);
    const unsigned lk = L_K + w * 4096, lv = L_V + w * 4096;
    BAR();
    DMA1(k0, ksrc, lk); DMA1O(k0 ^ 64, ksrc, lk, 1024); DMA1O(k0 ^ 128, ksrc, lk, 2048); DMA1O(k0 ^ 192, ksrc, lk, 3072);
    DMA1(va, vsrc, lv); DMA1O(va, vsrc, lv, 1024); DMA1O(va ^ 128, vsrc, lv, 2048); DMA1O(va ^ 128, vsrc, lv, 3072);
    const int f0 = (w < 7 ? w : 0) * 256 + lane * 4;
    gw1 = *(const GAS u32x2*)(w1 + f0); gb1 = *(const GAS u32x2*)(b1 + f0); gw2 = *(const GAS u32x2*)(w2 + f0);
  }
  STAMP(1);
  VMWAIT(7);
  STAMP(2);
  bf16x8 kf[4];
#pragma unroll
  for (int t = 0; t < 4; ++t) kf[t] = lds_ld<bf16x8>(L_K + w * 4096 + c * 256 + (((4 * t + G) ^ c) << 4));
  BAR();
  STAMP(3);
  const unsigned t32 = lds_ld<unsigned>(L_CT + 4), t8 = t32 & 0xffu;
  float sc[4];
  {
    f32x4 d = {0.f, 0.f, 0.f, 0.f};
#pragma unroll
    for (int t = 0; t < 4; ++t) d = mfma(kf[t], lds_ld<bf16x8>(L_QS + (c & 3) * 320 + t * 64 + G * 16), d);
    const unsigned r01 = pk(d[0], d[1]), r23 = pk(d[2], d[3]);
    constexpr float SCL = 0.08838834764831845f;
    const unsigned s01 = pk(lo16(r01) * SCL, hi16(r01) * SCL), s23 = pk(lo16(r23) * SCL, hi16(r23) * SCL);
    sc[0] = lo16(s01); sc[1] = hi16(s01); sc[2] = lo16(s23); sc[3] = hi16(s23);
    const float wm = sw32max(sw16max(fmaxf(fmaxf(sc[0], sc[1]), fmaxf(sc[2], sc[3]))));
    if (lane < 4) lds_st<float>(L_WM + lane * 32 + w * 4, wm);
  }
  BAR();
  {
    const f32x4 m0 = lds_ld<f32x4>(L_WM + (c & 3) * 32), m1 = lds_ld<f32x4>(L_WM + (c & 3) * 32 + 16);
    const float m = fmaxf(fmaxf(fmaxf(m0[0], m0[1]), fmaxf(m0[2], m0[3])), fmaxf(fmaxf(m1[0], m1[1]), fmaxf(m1[2], m1[3])));
    float p[4];
#pragma unroll
    for (int e = 0; e < 4; ++e) p[e] = __builtin_amdgcn_exp2f((sc[e] - m) * L2E);
    if (c < 4) lds_st<u32x2>(L_P + c * 320 + w * 32 + G * 8, u32x2{pk(p[0], p[1]), pk(p[2], p[3])});
  }
  STAMP(4);
  VMWAIT(3);
  STAMP(5);
  BAR();
  {
    const char* src = (const char*)wo + ((size_t)((h * 4 + g2) * 128 + w * 16) * 512 + cb * 64) * 2;
    const int r = lane >> 3, pos = lane & 7;
    const unsigned v0 = r * 1024 + ((((pos >> 1) ^ ((r >> 1) & 1)) << 1 | (pos & 1)) << 4);
    const unsigned l0 = L_WQ + w * 2048;
    DMA1(v0, src, l0); DMA1(v0 ^ 64, src + 8192, l0 + 1024);
  }
  {
    const int q = c >> 2, p = c & 3;
    const unsigned va = L_V + (8 * G + q) * 256 + (((2 * w + (p >> 1)) ^ ((q | ((G & 1) << 2)) << 1)) << 4) + (p & 1) * 8;
    f32x4 d = {0.f, 0.f, 0.f, 0.f};
#pragma unroll
    for (int t = 0; t < 4; ++t) d = mfma(trfrag(va + t * 8192, va + t * 8192 + 1024), lds_ld<bf16x8>(L_P + (c & 3) * 320 + t * 64 + G * 16), d);
    const unsigned off = c < 4 ? (unsigned)(W_O + ((h * 4 + c) * 32 + j) * 640 + w * 64 + G * 16) : 0x80000000u;
    __builtin_amdgcn_raw_buffer_store_b128(u32x4{tag8(d[0], t8), tag8(d[1], t8), tag8(d[2], t8), tag8(d[3], t8)}, rs, off, 0, 16);
  }
  STAMP(6);
  {
    const f32x2 hv = lds_ld<f32x2>(L_H + (64 * h + 2 * j) * 4);
    float a[2] = {0.f, 0.f};
#pragma unroll
    for (int d = 0; d < 2; ++d)
#pragma unroll
      for (int i = 0; i < 2; ++i) {
        const unsigned t1 = pk(hv[d] * lo16(gw1[i]), hv[d] * hi16(gw1[i]));
        const unsigned t2 = pk(lo16(t1) + lo16(gb1[i]), hi16(t1) + hi16(gb1[i]));
        float ge[2];
#pragma unroll
        for (int k = 0; k < 2; ++k) { const float u = k ? hi16(t2) : lo16(t2);
          const float z = u * fmaf(u * u, 0.044715f, 1.f) * (-2.f * 0.7978845608028654f * L2E);
          ge[k] = u * __builtin_amdgcn_rcpf(1.f + __builtin_amdgcn_exp2f(z)); }
        const unsigned t3 = pk(ge[0], ge[1]);
        a[d] = fmaf(lo16(t3), lo16(gw2[i]), a[d]); a[d] = fmaf(hi16(t3), hi16(gw2[i]), a[d]);
      }
    auto s = __builtin_amdgcn_permlane32_swap(__float_as_uint(a[0]), __float_as_uint(a[1]), false, false);
    const float r = sw16sum(sum16(__uint_as_float(s[0]) + __uint_as_float(s[1])));
    if ((lane & 31) == 0) lds_st<float>(L_GP + (lane >> 5) * 32 + w * 4, r);
  }
  STAMP(7);
  float invL;
  {
    const int half = lane >> 5, i = lane & 31;
    const unsigned rec = W_O + (h * 4 + g2) * 32 * 640;
    const unsigned oo = rec + (4 * w + half) * 640 + i * 16, om = rec + i * 640 + 512 + half * 8;
    u32x4 o0, o1; u32x2 ml;
    for (unsigned spins = 0; spins < SPIN_CAP; ++spins) {
      asm volatile("" ::: "memory");
      ml = __builtin_amdgcn_raw_buffer_load_b64(rs, om, 0, 16);
      o0 = __builtin_amdgcn_raw_buffer_load_b128(rs, oo, 0, 16);
      o1 = __builtin_amdgcn_raw_buffer_load_b128(rs, oo + 1280, 0, 16);
      if (__all((((bad4(o0, t8) | bad4(o1, t8)) & 0xffu) | (ml[1] ^ t32)) == 0)) break;
    }
    STAMP(8);
    auto s = __builtin_amdgcn_permlane32_swap(ml[0], ml[0], false, false);
    const float ms = __uint_as_float(s[0]), ls = __uint_as_float(s[1]);
    const float M = sw16max(max16(ms));
    const float wt = __builtin_amdgcn_exp2f((ms - M) * L2E);
    const float w0 = __builtin_bit_cast(float, __builtin_amdgcn_readlane(__float_as_int(wt), 4 * w)), w1_ = __builtin_bit_cast(float, __builtin_amdgcn_readlane(__float_as_int(wt), 4 * w + 1));
    const float w2_ = __builtin_bit_cast(float, __builtin_amdgcn_readlane(__float_as_int(wt), 4 * w + 2)), w3 = __builtin_bit_cast(float, __builtin_amdgcn_readlane(__float_as_int(wt), 4 * w + 3));
    const float wa = half ? w1_ : w0, wb = half ? w3 : w2_;
    float a[4];
#pragma unroll
    for (int e = 0; e < 4; ++e) a[e] = fmaf(wa, __uint_as_float(o0[e]), wb * __uint_as_float(o1[e]));
    lds_st<u32x2>(L_OA + (2 * w + half) * 272 + i * 8, u32x2{pk(a[0], a[1]), pk(a[2], a[3])});
    invL = __builtin_amdgcn_rcpf(sw16sum(sum16(wt * ls)));
  }
  VMWAIT(0);
  BAR();
  STAMP(9);
  if (w < 4) {
    const int q = c >> 2, p = c & 3, s = ((q >> 1) & 1) | ((G & 1) << 1);
    const unsigned wa = L_WQ + (8 * G + q) * 128 + ((w ^ s) << 5) + (p >> 1) * 16 + (p & 1) * 8;
    f32x4 d = {0.f, 0.f, 0.f, 0.f};
#pragma unroll
    for (int t = 0; t < 4; ++t) d = mfma(lds_ld<bf16x8>(L_OA + c * 272 + t * 64 + G * 16), trfrag(wa + t * 4096, wa + t * 4096 + 512), d);
    const float r = sw32sum(sw16sum((d[0] + d[1]) + (d[2] + d[3]))) * invL;
    const unsigned off = lane < 16 ? (unsigned)(W_F + ((cb * 32 + h * 4 + g2) * 64 + w * 16 + c) * 8) : 0x80000000u;
    __builtin_amdgcn_raw_buffer_store_b64(u32x2{__float_as_uint(r), t32}, rs, off, 0, 16);
    STAMP(10);
  }
}

extern "C" void kernel_launch(void* const* d_in, const int* in_sizes, int n_in, void* d_out, int out_size, void* d_ws, size_t ws_size, hipStream_t stream) {
  (void)hipFuncSetAttribute((const void*)fused, hipFuncAttributeMaxDynamicSharedMemorySize, L_END);
  fused<<<256, 768, L_END, stream>>>((const u16*)d_in[0], (const u16*)d_in[1], (const u16*)d_in[2], (const u16*)d_in[3], (const u16*)d_in[4],
                                     (const u16*)d_in[6], (const u16*)d_in[7], (const u16*)d_in[8], (const u16*)d_in[9], (u16*)d_out, (char*)d_ws);
}
```

```cpp
#include <hip/hip_runtime.h>
#include <hip/hip_bf16.h>
#include <stdint.h>

typedef unsigned short u16;
typedef unsigned long long u64;
typedef __attribute__((ext_vector_type(2))) unsigned u32x2;
typedef __attribute__((ext_vector_type(4))) unsigned u32x4;
typedef __attribute__((ext_vector_type(2))) float f32x2;
typedef __attribute__((ext_vector_type(4))) float f32x4;
typedef __attribute__((ext_vector_type(2))) __bf16 bf16x2;
typedef __attribute__((ext_vector_type(4))) __bf16 bf16x4;
typedef __attribute__((ext_vector_type(8))) __bf16 bf16x8;
#define LAS __attribute__((address_space(3)))
#define GAS __attribute__((address_space(1)))

constexpr int SK = 32768, WIN = 4096;
constexpr int L_WQ = 0;
constexpr int L_K = 16384;
constexpr int L_V = 49152;
constexpr int L_QP = 81920;
constexpr int L_QS = L_QP + 1024;
constexpr int L_P = L_QS + 1280;
constexpr int L_OA = L_P + 1280;
constexpr int L_H = L_OA + 4352;
constexpr int L_WM = L_H + 2048;
constexpr int L_GP = L_WM + 128;
constexpr int L_CT = L_GP + 64;
constexpr int L_END = L_CT + 16;
constexpr int W_EP = 0;
constexpr int W_Q = 4096;
constexpr int W_O = W_Q + 65536;
constexpr int W_F = W_O + 8 * 4 * 32 * 640;
constexpr int W_END = W_F + 131072;
constexpr int W_ST = 1 << 20;
constexpr unsigned SPIN_CAP = 1u << 16;
constexpr float L2E = 1.4426950408889634f;

#define STAMP(i) do {} while (0)

__device__ __forceinline__ unsigned pk(float a, float b) { f32x2 v = {a, b}; return __builtin_bit_cast(unsigned, __builtin_convertvector(v, bf16x2)); }
__device__ __forceinline__ float lo16(unsigned u) { return __uint_as_float(u << 16); }
__device__ __forceinline__ float hi16(unsigned u) { return __uint_as_float(u & 0xffff0000u); }
__device__ __forceinline__ float rbf(float a) { return lo16(pk(a, a)); }
template <int CTRL> __device__ __forceinline__ float dpp(float x) {
  return __builtin_bit_cast(float, __builtin_amdgcn_mov_dpp(__builtin_bit_cast(int, x), CTRL, 0xf, 0xf, true));
}
constexpr int XOR1 = 0xB1, XOR2 = 0x4E, HMIR = 0x141, ROR8 = 0x128;
__device__ __forceinline__ float sum16(float x) { x += dpp<XOR1>(x); x += dpp<XOR2>(x); x += dpp<HMIR>(x); x += dpp<ROR8>(x); return x; }
__device__ __forceinline__ float max16(float x) { x = fmaxf(x, dpp<XOR1>(x)); x = fmaxf(x, dpp<XOR2>(x)); x = fmaxf(x, dpp<HMIR>(x)); x = fmaxf(x, dpp<ROR8>(x)); return x; }
__device__ __forceinline__ float sw16sum(float x) { auto s = __builtin_amdgcn_permlane16_swap(__float_as_uint(x), __float_as_uint(x), false, false); return __uint_as_float(s[0]) + __uint_as_float(s[1]); }
__device__ __forceinline__ float sw32sum(float x) { auto s = __builtin_amdgcn_permlane32_swap(__float_as_uint(x), __float_as_uint(x), false, false); return __uint_as_float(s[0]) + __uint_as_float(s[1]); }
__device__ __forceinline__ float sw16max(float x) { auto s = __builtin_amdgcn_permlane16_swap(__float_as_uint(x), __float_as_uint(x), false, false); return fmaxf(__uint_as_float(s[0]), __uint_as_float(s[1])); }
__device__ __forceinline__ float sw32max(float x) { auto s = __builtin_amdgcn_permlane32_swap(__float_as_uint(x), __float_as_uint(x), false, false); return fmaxf(__uint_as_float(s[0]), __uint_as_float(s[1])); }
__device__ __forceinline__ float sum64(float x) { return sw32sum(sw16sum(sum16(x))); }

#define BAR() asm volatile("s_waitcnt lgkmcnt(0)\n\ts_barrier" ::: "memory")
#define VMWAIT(n) asm volatile("s_waitcnt vmcnt(" #n ")" ::: "memory")
#define DMA1(voff, sbase, ldsb) asm volatile("s_mov_b32 m0, %2\n\ts_nop 0\n\tglobal_load_lds_dwordx4 %0, %1 nt" :: "v"(voff), "s"(sbase), "s"(ldsb) : "memory")
#define DMA1O(voff, sbase, ldsb, OFF) asm volatile("s_mov_b32 m0, %2\n\ts_nop 0\n\tglobal_load_lds_dwordx4 %0, %1 offset:" #OFF " nt" :: "v"(voff), "s"(sbase), "s"(ldsb) : "memory")

template <typename T> __device__ __forceinline__ T lds_ld(unsigned a) { return *(LAS T*)(size_t)a; }
template <typename T> __device__ __forceinline__ void lds_st(unsigned a, T v) { *(LAS T*)(size_t)a = v; }
__device__ __forceinline__ bf16x8 trfrag(unsigned a0, unsigned a1) {
  bf16x4 a = __builtin_amdgcn_ds_read_tr16_b64_v4bf16((LAS bf16x4*)(size_t)a0);
  bf16x4 c = __builtin_amdgcn_ds_read_tr16_b64_v4bf16((LAS bf16x4*)(size_t)a1);
  return __builtin_shufflevector(a, c, 0, 1, 2, 3, 4, 5, 6, 7);
}
__device__ __forceinline__ f32x4 mfma(bf16x8 a, bf16x8 b, f32x4 c) { return __builtin_amdgcn_mfma_f32_16x16x32_bf16(a, b, c, 0, 0, 0); }
__device__ __forceinline__ unsigned tag8(float v, unsigned t8) { return (__float_as_uint(v) & 0xffffff00u) | t8; }
__device__ __forceinline__ unsigned bad4(u32x4 v, unsigned t8) { return ((v[0] ^ t8) | (v[1] ^ t8) | (v[2] ^ t8) | (v[3] ^ t8)); }

__device__ __forceinline__ void ln_stats(const float (&v)[8], float& mb, float& den) {
  float s = ((v[0] + v[1]) + (v[2] + v[3])) + ((v[4] + v[5]) + (v[6] + v[7]));
  s = sum64(s); const float mean = s * (1.f / 512.f);
  float q = 0.f;
#pragma unroll
  for (int i = 0; i < 8; ++i) { const float d = v[i] - mean; q = fmaf(d, d, q); }
  q = sum64(q);
  mb = rbf(mean);
  den = rbf(__builtin_sqrtf(rbf(rbf(q * (1.f / 512.f)) + 1e-5f)));
}
__device__ __forceinline__ float ln_apply(float x, float mb, float den) { return rbf(x - mb) / den; }

__global__ void __launch_bounds__(768) fused(const u16* __restrict__ x, const u16* __restrict__ kc, const u16* __restrict__ vc,
                                             const u16* __restrict__ wq, const u16* __restrict__ wo, const u16* __restrict__ w1,
                                             const u16* __restrict__ b1, const u16* __restrict__ w2, const u16* __restrict__ b2,
                                             u16* __restrict__ out, char* ws) {
  u64 tfirst; asm volatile("s_memrealtime %0" : "=s"(tfirst));
  extern __shared__ __attribute__((aligned(16))) char smem[];
  const int b = blockIdx.x, h = b & 7, j = b >> 3;
  const int lane = threadIdx.x & 63, wave = __builtin_amdgcn_readfirstlane(threadIdx.x >> 6);
  const int G = lane >> 4, c = lane & 15;
  const __amdgpu_buffer_rsrc_t rs = __builtin_amdgcn_make_buffer_rsrc(ws, 0, W_END, 0x00020000);
  asm volatile("s_waitcnt lgkmcnt(0)" : "+s"(tfirst) : "s"(ws), "s"(x) : "memory");
  if (wave == 0 && lane == 0) __hip_atomic_store((GAS u64*)(ws + W_ST) + b, tfirst, __ATOMIC_RELAXED, __HIP_MEMORY_SCOPE_AGENT);
  if (wave < 4) {
    __builtin_amdgcn_s_setprio(3);
    const int ks = j & 3, cblk = j >> 2;
    const u32x4 xv = *(const GAS u32x4*)(x + lane * 8);
    const u32x4 xf = *(const GAS u32x4*)(x + ks * 128 + wave * 32 + G * 8);
    {
      const char* src = (const char*)wq + ((size_t)(ks * 128 + wave * 32) * 4096 + h * 512 + cblk * 64) * 2;
      const int r = lane >> 3, pos = lane & 7;
      const unsigned v0 = r * 8192 + ((((pos >> 1) ^ ((r >> 1) & 1)) << 1 | (pos & 1)) << 4);
      const unsigned v1 = v0 ^ 64;
      const unsigned l0 = L_WQ + wave * 4096;
      DMA1(v0, src, l0); DMA1(v1, src + 65536, l0 + 1024); DMA1(v0, src + 131072, l0 + 2048); DMA1(v1, src + 196608, l0 + 3072);
    }
    unsigned e32 = 0, xo = 0;
    if (wave == 0) {
      e32 = __hip_atomic_load((GAS unsigned*)(ws + W_EP), __ATOMIC_RELAXED, __HIP_MEMORY_SCOPE_AGENT);
      xo = *(const GAS unsigned*)(x + 64 * h + 2 * j);
      if (lane == 0) lds_st<unsigned>(L_CT, 0u);
    }
    BAR();
    STAMP(1);
    float v[8];
#pragma unroll
    for (int i = 0; i < 4; ++i) { v[2 * i] = lo16(xv[i]); v[2 * i + 1] = hi16(xv[i]); }
    float mb, den; ln_stats(v, mb, den);
    u32x4 af;
#pragma unroll
    for (int i = 0; i < 4; ++i) af[i] = pk(ln_apply(lo16(xf[i]), mb, den), ln_apply(hi16(xf[i]), mb, den));
    const unsigned t32 = e32 + 0x9E3779B1u;
    if (wave == 0 && lane == 0) lds_st<unsigned>(L_CT + 4, t32);
    STAMP(2);
    VMWAIT(0);
    STAMP(3);
    f32x4 acc[4];
    {
      const int q = c >> 2, p = c & 3, row = 8 * G + q, s = ((q >> 1) & 1) | ((G & 1) << 1);
      const unsigned base = L_WQ + wave * 4096 + row * 128 + (p >> 1) * 16 + (p & 1) * 8;
#pragma unroll
      for (int t = 0; t < 4; ++t) { const unsigned a = base + ((t ^ s) << 5);
        acc[t] = mfma(__builtin_bit_cast(bf16x8, af), trfrag(a, a + 512), f32x4{0.f, 0.f, 0.f, 0.f}); }
    }
    const float qp = G == 0 ? acc[0][0] : G == 1 ? acc[1][0] : G == 2 ? acc[2][0] : acc[3][0];
    lds_st<float>(L_QP + wave * 256 + lane * 4, qp);
    asm volatile("s_waitcnt lgkmcnt(0)" ::: "memory");
    unsigned old = 0;
    if (lane == 0) old = __hip_atomic_fetch_add((LAS unsigned*)(size_t)L_CT, 1u, __ATOMIC_RELAXED, __HIP_MEMORY_SCOPE_WORKGROUP);
    old = __builtin_amdgcn_readfirstlane(old);
    asm volatile("" ::: "memory");
    if (old == 3) {
      const unsigned t8 = lds_ld<unsigned>(L_CT + 4) & 0xffu;
      const unsigned a = L_QP + c * 16;
      const f32x4 s = (lds_ld<f32x4>(a) + lds_ld<f32x4>(a + 256)) + (lds_ld<f32x4>(a + 512) + lds_ld<f32x4>(a + 768));
      const u32x4 o = {tag8(s[0], t8), tag8(s[1], t8), tag8(s[2], t8), tag8(s[3], t8)};
      const unsigned off = lane < 16 ? (unsigned)(W_Q + ((h * 4 + ks) * 512 + cblk * 64) * 4 + c * 16) : 0x80000000u;
      __builtin_amdgcn_raw_buffer_store_b128(o, rs, off, 0, 16);
      STAMP(4);
    }
    if (wave == 1) {
      float u[8];
#pragma unroll
      for (int i = 0; i < 8; ++i) u[i] = rbf(ln_apply(v[i], mb, den));
      float mb2, den2; ln_stats(u, mb2, den2);
      f32x4 h0, h1;
#pragma unroll
      for (int i = 0; i < 4; ++i) { h0[i] = rbf(ln_apply(u[i], mb2, den2)); h1[i] = rbf(ln_apply(u[4 + i], mb2, den2)); }
      lds_st<f32x4>(L_H + lane * 32, h0); lds_st<f32x4>(L_H + lane * 32 + 16, h1);
    }
    if (wave != 0) return;
    const unsigned t8 = t32 & 0xffu;
    {
      const unsigned off = W_Q + h * 8192 + lane * 32;
      u32x4 g[8];
      for (unsigned spins = 0; spins < SPIN_CAP; ++spins) {
        asm volatile("" ::: "memory");
#pragma unroll
        for (int k = 0; k < 4; ++k) { g[2 * k] = __builtin_amdgcn_raw_buffer_load_b128(rs, off + k * 2048, 0, 16); g[2 * k + 1] = __builtin_amdgcn_raw_buffer_load_b128(rs, off + k * 2048 + 16, 0, 16); }
        unsigned bad = 0;
#pragma unroll
        for (int k = 0; k < 8; ++k) bad |= bad4(g[k], t8);
        if (__all((bad & 0xffu) == 0)) break;
      }
      float q[8];
#pragma unroll
      for (int e = 0; e < 8; ++e) q[e] = (__uint_as_float(g[e >> 2][e & 3]) + __uint_as_float(g[2 + (e >> 2)][e & 3])) + (__uint_as_float(g[4 + (e >> 2)][e & 3]) + __uint_as_float(g[6 + (e >> 2)][e & 3]));
      lds_st<u32x4>(L_QS + G * 320 + c * 16, u32x4{pk(q[0], q[1]), pk(q[2], q[3]), pk(q[4], q[5]), pk(q[6], q[7])});
    }
    STAMP(5);
    BAR();
    BAR();
    const f32x4 m0 = lds_ld<f32x4>(L_WM + (c & 3) * 32), m1 = lds_ld<f32x4>(L_WM + (c & 3) * 32 + 16);
    const float m = fmaxf(fmaxf(fmaxf(m0[0], m0[1]), fmaxf(m0[2], m0[3])), fmaxf(fmaxf(m1[0], m1[1]), fmaxf(m1[2], m1[3])));
    BAR();
    {
      f32x4 l = {0.f, 0.f, 0.f, 0.f};
      const u32x4 ones = {0x3f803f80u, 0x3f803f80u, 0x3f803f80u, 0x3f803f80u};
#pragma unroll
      for (int t = 0; t < 4; ++t) l = mfma(__builtin_bit_cast(bf16x8, ones), lds_ld<bf16x8>(L_P + (c & 3) * 320 + t * 64 + G * 16), l);
      const unsigned off = lane < 4 ? (unsigned)(W_O + ((h * 4 + lane) * 32 + j) * 640 + 512) : 0x80000000u;
      __builtin_amdgcn_raw_buffer_store_b128(u32x4{__float_as_uint(m), t32, __float_as_uint(l[0]), t32}, rs, off, 0, 16);
    }
    STAMP(6);
    BAR();
    const f32x4 g0 = lds_ld<f32x4>(L_GP + (lane >> 5) * 32), g1 = lds_ld<f32x4>(L_GP + (lane >> 5) * 32 + 16);
    const float ys = ((g0[0] + g0[1]) + (g0[2] + g0[3])) + ((g1[0] + g1[1]) + g1[2]);
    const float y = rbf(rbf(ys) + lo16(*(const GAS u16*)b2));
    const float gate = rbf(y * __builtin_amdgcn_rcpf(1.f + __builtin_amdgcn_exp2f(-L2E * y)));
    u32x2 f;
    {
      const unsigned off = W_F + ((h * 32 + (lane & 31)) * 64 + 2 * j + (lane >> 5)) * 8;
      for (unsigned spins = 0; spins < SPIN_CAP; ++spins) {
        asm volatile("" ::: "memory");
        f = __builtin_amdgcn_raw_buffer_load_b64(rs, off, 0, 16);
        if (__all(f[1] == t32)) break;
      }
    }
    STAMP(7);
    const float s = rbf(sw16sum(sum16(__uint_as_float(f[0]))));
    const float xs = lane < 32 ? lo16(xo) : hi16(xo);
    const float o = xs + rbf(s * gate);
    if ((lane & 31) == 0) out[64 * h + 2 * j + (lane >> 5)] = (u16)(pk(o, o) & 0xffffu);
    if (b == 0) { u64 mn = ~0ull, mx = 0; for (int k = 0; k < 256; ++k) { u64 t = __hip_atomic_load((GAS u64*)(ws + W_ST) + k, __ATOMIC_RELAXED, __HIP_MEMORY_SCOPE_AGENT); mn = t < mn ? t : mn; mx = t > mx ? t : mx; } u64 d = tfirst - mn; d = d < 5000 ? d * 20 : 0; d = __builtin_amdgcn_readfirstlane((unsigned)d); { const u64 c0 = __builtin_amdgcn_s_memrealtime(); while (__builtin_amdgcn_s_memrealtime() - c0 < d) __builtin_amdgcn_s_sleep(1); } }
    if (b == 0 && lane == 0) __hip_atomic_store((GAS unsigned*)(ws + W_EP), t32, __ATOMIC_RELAXED, __HIP_MEMORY_SCOPE_AGENT);
    STAMP(8);
    return;
  }
  const int w = wave - 4;
  const int g2 = j >> 3, cb = j & 7;
  u32x2 gw1, gb1, gw2;
  {
    const size_t rowoff = ((size_t)h * SK + (SK - WIN) + j * 128 + w * 16) * 256;
    const char* ksrc = (const char*)kc + rowoff; const char* vsrc = (const char*)vc + rowoff;
    const unsigned k0 = G * 256 + ((c ^ G) << 4);
    const unsigned va = G * 256 + ((c ^ (G << 1)) << 4);
    const unsigned lk = L_K + w * 4096, lv = L_V + w * 4096;
    BAR();
    DMA1(k0, ksrc, lk); DMA1O(k0 ^ 64, ksrc, lk, 1024); DMA1O(k0 ^ 128, ksrc, lk, 2048); DMA1O(k0 ^ 192, ksrc, lk, 3072);
    DMA1(va, vsrc, lv); DMA1O(va, vsrc, lv, 1024); DMA1O(va ^ 128, vsrc, lv, 2048); DMA1O(va ^ 128, vsrc, lv, 3072);
    const int f0 = (w < 7 ? w : 0) * 256 + lane * 4;
    gw1 = *(const GAS u32x2*)(w1 + f0); gb1 = *(const GAS u32x2*)(b1 + f0); gw2 = *(const GAS u32x2*)(w2 + f0);
  }
  STAMP(1);
  VMWAIT(7);
  STAMP(2);
  bf16x8 kf[4];
#pragma unroll
  for (int t = 0; t < 4; ++t) kf[t] = lds_ld<bf16x8>(L_K + w * 4096 + c * 256 + (((4 * t + G) ^ c) << 4));
  BAR();
  STAMP(3);
  const unsigned t32 = lds_ld<unsigned>(L_CT + 4), t8 = t32 & 0xffu;
  float sc[4];
  {
    f32x4 d = {0.f, 0.f, 0.f, 0.f};
#pragma unroll
    for (int t = 0; t < 4; ++t) d = mfma(kf[t], lds_ld<bf16x8>(L_QS + (c & 3) * 320 + t * 64 + G * 16), d);
    const unsigned r01 = pk(d[0], d[1]), r23 = pk(d[2], d[3]);
    constexpr float SCL = 0.08838834764831845f;
    const unsigned s01 = pk(lo16(r01) * SCL, hi16(r01) * SCL), s23 = pk(lo16(r23) * SCL, hi16(r23) * SCL);
    sc[0] = lo16(s01); sc[1] = hi16(s01); sc[2] = lo16(s23); sc[3] = hi16(s23);
    const float wm = sw32max(sw16max(fmaxf(fmaxf(sc[0], sc[1]), fmaxf(sc[2], sc[3]))));
    if (lane < 4) lds_st<float>(L_WM + lane * 32 + w * 4, wm);
  }
  BAR();
  {
    const f32x4 m0 = lds_ld<f32x4>(L_WM + (c & 3) * 32), m1 = lds_ld<f32x4>(L_WM + (c & 3) * 32 + 16);
    const float m = fmaxf(fmaxf(fmaxf(m0[0], m0[1]), fmaxf(m0[2], m0[3])), fmaxf(fmaxf(m1[0], m1[1]), fmaxf(m1[2], m1[3])));
    float p[4];
#pragma unroll
    for (int e = 0; e < 4; ++e) p[e] = __builtin_amdgcn_exp2f((sc[e] - m) * L2E);
    if (c < 4) lds_st<u32x2>(L_P + c * 320 + w * 32 + G * 8, u32x2{pk(p[0], p[1]), pk(p[2], p[3])});
  }
  STAMP(4);
  VMWAIT(3);
  STAMP(5);
  BAR();
  {
    const char* src = (const char*)wo + ((size_t)((h * 4 + g2) * 128 + w * 16) * 512 + cb * 64) * 2;
    const int r = lane >> 3, pos = lane & 7;
    const unsigned v0 = r * 1024 + ((((pos >> 1) ^ ((r >> 1) & 1)) << 1 | (pos & 1)) << 4);
    const unsigned l0 = L_WQ + w * 2048;
    DMA1(v0, src, l0); DMA1(v0 ^ 64, src + 8192, l0 + 1024);
  }
  {
    const int q = c >> 2, p = c & 3;
    const unsigned va = L_V + (8 * G + q) * 256 + (((2 * w + (p >> 1)) ^ ((q | ((G & 1) << 2)) << 1)) << 4) + (p & 1) * 8;
    f32x4 d = {0.f, 0.f, 0.f, 0.f};
#pragma unroll
    for (int t = 0; t < 4; ++t) d = mfma(trfrag(va + t * 8192, va + t * 8192 + 1024), lds_ld<bf16x8>(L_P + (c & 3) * 320 + t * 64 + G * 16), d);
    const unsigned off = c < 4 ? (unsigned)(W_O + ((h * 4 + c) * 32 + j) * 640 + w * 64 + G * 16) : 0x80000000u;
    __builtin_amdgcn_raw_buffer_store_b128(u32x4{tag8(d[0], t8), tag8(d[1], t8), tag8(d[2], t8), tag8(d[3], t8)}, rs, off, 0, 16);
  }
  STAMP(6);
  {
    const f32x2 hv = lds_ld<f32x2>(L_H + (64 * h + 2 * j) * 4);
    float a[2] = {0.f, 0.f};
#pragma unroll
    for (int d = 0; d < 2; ++d)
#pragma unroll
      for (int i = 0; i < 2; ++i) {
        const unsigned t1 = pk(hv[d] * lo16(gw1[i]), hv[d] * hi16(gw1[i]));
        const unsigned t2 = pk(lo16(t1) + lo16(gb1[i]), hi16(t1) + hi16(gb1[i]));
        float ge[2];
#pragma unroll
        for (int k = 0; k < 2; ++k) { const float u = k ? hi16(t2) : lo16(t2);
          const float z = u * fmaf(u * u, 0.044715f, 1.f) * (-2.f * 0.7978845608028654f * L2E);
          ge[k] = u * __builtin_amdgcn_rcpf(1.f + __builtin_amdgcn_exp2f(z)); }
        const unsigned t3 = pk(ge[0], ge[1]);
        a[d] = fmaf(lo16(t3), lo16(gw2[i]), a[d]); a[d] = fmaf(hi16(t3), hi16(gw2[i]), a[d]);
      }
    auto s = __builtin_amdgcn_permlane32_swap(__float_as_uint(a[0]), __float_as_uint(a[1]), false, false);
    const float r = sw16sum(sum16(__uint_as_float(s[0]) + __uint_as_float(s[1])));
    if ((lane & 31) == 0) lds_st<float>(L_GP + (lane >> 5) * 32 + w * 4, r);
  }
  STAMP(7);
  float invL;
  {
    const int half = lane >> 5, i = lane & 31;
    const unsigned rec = W_O + (h * 4 + g2) * 32 * 640;
    const unsigned oo = rec + (4 * w + half) * 640 + i * 16, om = rec + i * 640 + 512 + half * 8;
    u32x4 o0, o1; u32x2 ml;
    for (unsigned spins = 0; spins < SPIN_CAP; ++spins) {
      asm volatile("" ::: "memory");
      ml = __builtin_amdgcn_raw_buffer_load_b64(rs, om, 0, 16);
      o0 = __builtin_amdgcn_raw_buffer_load_b128(rs, oo, 0, 16);
      o1 = __builtin_amdgcn_raw_buffer_load_b128(rs, oo + 1280, 0, 16);
      if (__all((((bad4(o0, t8) | bad4(o1, t8)) & 0xffu) | (ml[1] ^ t32)) == 0)) break;
    }
    STAMP(8);
    auto s = __builtin_amdgcn_permlane32_swap(ml[0], ml[0], false, false);
    const float ms = __uint_as_float(s[0]), ls = __uint_as_float(s[1]);
    const float M = sw16max(max16(ms));
    const float wt = __builtin_amdgcn_exp2f((ms - M) * L2E);
    const float w0 = __builtin_bit_cast(float, __builtin_amdgcn_readlane(__float_as_int(wt), 4 * w)), w1_ = __builtin_bit_cast(float, __builtin_amdgcn_readlane(__float_as_int(wt), 4 * w + 1));
    const float w2_ = __builtin_bit_cast(float, __builtin_amdgcn_readlane(__float_as_int(wt), 4 * w + 2)), w3 = __builtin_bit_cast(float, __builtin_amdgcn_readlane(__float_as_int(wt), 4 * w + 3));
    const float wa = half ? w1_ : w0, wb = half ? w3 : w2_;
    float a[4];
#pragma unroll
    for (int e = 0; e < 4; ++e) a[e] = fmaf(wa, __uint_as_float(o0[e]), wb * __uint_as_float(o1[e]));
    lds_st<u32x2>(L_OA + (2 * w + half) * 272 + i * 8, u32x2{pk(a[0], a[1]), pk(a[2], a[3])});
    invL = __builtin_amdgcn_rcpf(sw16sum(sum16(wt * ls)));
  }
  VMWAIT(0);
  BAR();
  STAMP(9);
  if (w < 4) {
    const int q = c >> 2, p = c & 3, s = ((q >> 1) & 1) | ((G & 1) << 1);
    const unsigned wa = L_WQ + (8 * G + q) * 128 + ((w ^ s) << 5) + (p >> 1) * 16 + (p & 1) * 8;
    f32x4 d = {0.f, 0.f, 0.f, 0.f};
#pragma unroll
    for (int t = 0; t < 4; ++t) d = mfma(lds_ld<bf16x8>(L_OA + c * 272 + t * 64 + G * 16), trfrag(wa + t * 4096, wa + t * 4096 + 512), d);
    const float r = sw32sum(sw16sum((d[0] + d[1]) + (d[2] + d[3]))) * invL;
    const unsigned off = lane < 16 ? (unsigned)(W_F + ((cb * 32 + h * 4 + g2) * 64 + w * 16 + c) * 8) : 0x80000000u;
    __builtin_amdgcn_raw_buffer_store_b64(u32x2{__float_as_uint(r), t32}, rs, off, 0, 16);
    STAMP(10);
  }
}

extern "C" void kernel_launch(void* const* d_in, const int* in_sizes, int n_in, void* d_out, int out_size, void* d_ws, size_t ws_size, hipStream_t stream) {
  (void)hipFuncSetAttribute((const void*)fused, hipFuncAttributeMaxDynamicSharedMemorySize, L_END);
  fused<<<256, 768, L_END, stream>>>((const u16*)d_in[0], (const u16*)d_in[1], (const u16*)d_in[2], (const u16*)d_in[3], (const u16*)d_in[4],
                                     (const u16*)d_in[6], (const u16*)d_in[7], (const u16*)d_in[8], (const u16*)d_in[9], (u16*)d_out, (char*)d_ws);
}
```

```cpp
#include <hip/hip_runtime.h>
#include <hip/hip_bf16.h>
#include <stdint.h>

typedef unsigned short u16;
typedef unsigned long long u64;
typedef __attribute__((ext_vector_type(2))) unsigned u32x2;
typedef __attribute__((ext_vector_type(4))) unsigned u32x4;
typedef __attribute__((ext_vector_type(2))) float f32x2;
typedef __attribute__((ext_vector_type(4))) float f32x4;
typedef __attribute__((ext_vector_type(2))) __bf16 bf16x2;
typedef __attribute__((ext_vector_type(4))) __bf16 bf16x4;
typedef __attribute__((ext_vector_type(8))) __bf16 bf16x8;
#define LAS __attribute__((address_space(3)))
#define GAS __attribute__((address_space(1)))

constexpr int SK = 32768, WIN = 4096;
constexpr int L_WQ = 0;
constexpr int L_K = 16384;
constexpr int L_V = 49152;
constexpr int L_QP = 81920;
constexpr int L_QS = L_QP + 1024;
constexpr int L_P = L_QS + 1280;
constexpr int L_OA = L_P + 1280;
constexpr int L_H = L_OA + 4352;
constexpr int L_WM = L_H + 2048;
constexpr int L_GP = L_WM + 128;
constexpr int L_CT = L_GP + 64;
constexpr int L_END = L_CT + 16;
constexpr int W_EP = 0;
constexpr int W_Q = 4096;
constexpr int W_O = W_Q + 65536;
constexpr int W_F = W_O + 8 * 4 * 32 * 640;
constexpr int W_END = W_F + 131072;
constexpr int W_ST = 1 << 20;
constexpr unsigned SPIN_CAP = 1u << 16;
constexpr float L2E = 1.4426950408889634f;

#define STAMP(i) do {} while (0)

__device__ __forceinline__ unsigned pk(float a, float b) { f32x2 v = {a, b}; return __builtin_bit_cast(unsigned, __builtin_convertvector(v, bf16x2)); }
__device__ __forceinline__ float lo16(unsigned u) { return __uint_as_float(u << 16); }
__device__ __forceinline__ float hi16(unsigned u) { return __uint_as_float(u & 0xffff0000u); }
__device__ __forceinline__ float rbf(float a) { return lo16(pk(a, a)); }
template <int CTRL> __device__ __forceinline__ float dpp(float x) {
  return __builtin_bit_cast(float, __builtin_amdgcn_mov_dpp(__builtin_bit_cast(int, x), CTRL, 0xf, 0xf, true));
}
constexpr int XOR1 = 0xB1, XOR2 = 0x4E, HMIR = 0x141, ROR8 = 0x128;
__device__ __forceinline__ float sum16(float x) { x += dpp<XOR1>(x); x += dpp<XOR2>(x); x += dpp<HMIR>(x); x += dpp<ROR8>(x); return x; }
__device__ __forceinline__ float max16(float x) { x = fmaxf(x, dpp<XOR1>(x)); x = fmaxf(x, dpp<XOR2>(x)); x = fmaxf(x, dpp<HMIR>(x)); x = fmaxf(x, dpp<ROR8>(x)); return x; }
__device__ __forceinline__ float sw16sum(float x) { auto s = __builtin_amdgcn_permlane16_swap(__float_as_uint(x), __float_as_uint(x), false, false); return __uint_as_float(s[0]) + __uint_as_float(s[1]); }
__device__ __forceinline__ float sw32sum(float x) { auto s = __builtin_amdgcn_permlane32_swap(__float_as_uint(x), __float_as_uint(x), false, false); return __uint_as_float(s[0]) + __uint_as_float(s[1]); }
__device__ __forceinline__ float sw16max(float x) { auto s = __builtin_amdgcn_permlane16_swap(__float_as_uint(x), __float_as_uint(x), false, false); return fmaxf(__uint_as_float(s[0]), __uint_as_float(s[1])); }
__device__ __forceinline__ float sw32max(float x) { auto s = __builtin_amdgcn_permlane32_swap(__float_as_uint(x), __float_as_uint(x), false, false); return fmaxf(__uint_as_float(s[0]), __uint_as_float(s[1])); }
__device__ __forceinline__ float sum64(float x) { return sw32sum(sw16sum(sum16(x))); }

#define BAR() asm volatile("s_waitcnt lgkmcnt(0)\n\ts_barrier" ::: "memory")
#define VMWAIT(n) asm volatile("s_waitcnt vmcnt(" #n ")" ::: "memory")
#define DMA1(voff, sbase, ldsb) asm volatile("s_mov_b32 m0, %2\n\ts_nop 0\n\tglobal_load_lds_dwordx4 %0, %1 nt" :: "v"(voff), "s"(sbase), "s"(ldsb) : "memory")
#define DMA1O(voff, sbase, ldsb, OFF) asm volatile("s_mov_b32 m0, %2\n\ts_nop 0\n\tglobal_load_lds_dwordx4 %0, %1 offset:" #OFF " nt" :: "v"(voff), "s"(sbase), "s"(ldsb) : "memory")

template <typename T> __device__ __forceinline__ T lds_ld(unsigned a) { return *(LAS T*)(size_t)a; }
template <typename T> __device__ __forceinline__ void lds_st(unsigned a, T v) { *(LAS T*)(size_t)a = v; }
__device__ __forceinline__ bf16x8 trfrag(unsigned a0, unsigned a1) {
  bf16x4 a = __builtin_amdgcn_ds_read_tr16_b64_v4bf16((LAS bf16x4*)(size_t)a0);
  bf16x4 c = __builtin_amdgcn_ds_read_tr16_b64_v4bf16((LAS bf16x4*)(size_t)a1);
  return __builtin_shufflevector(a, c, 0, 1, 2, 3, 4, 5, 6, 7);
}
__device__ __forceinline__ f32x4 mfma(bf16x8 a, bf16x8 b, f32x4 c) { return __builtin_amdgcn_mfma_f32_16x16x32_bf16(a, b, c, 0, 0, 0); }
__device__ __forceinline__ unsigned tag8(float v, unsigned t8) { return (__float_as_uint(v) & 0xffffff00u) | t8; }
__device__ __forceinline__ unsigned bad4(u32x4 v, unsigned t8) { return ((v[0] ^ t8) | (v[1] ^ t8) | (v[2] ^ t8) | (v[3] ^ t8)); }

__device__ __forceinline__ void ln_stats(const float (&v)[8], float& mb, float& den) {
  float s = ((v[0] + v[1]) + (v[2] + v[3])) + ((v[4] + v[5]) + (v[6] + v[7]));
  s = sum64(s); const float mean = s * (1.f / 512.f);
  float q = 0.f;
#pragma unroll
  for (int i = 0; i < 8; ++i) { const float d = v[i] - mean; q = fmaf(d, d, q); }
  q = sum64(q);
  mb = rbf(mean);
  den = rbf(__builtin_sqrtf(rbf(rbf(q * (1.f / 512.f)) + 1e-5f)));
}
__device__ __forceinline__ float ln_apply(float x, float mb, float den) { return rbf(x - mb) / den; }

__global__ void __launch_bounds__(768) fused(const u16* __restrict__ x, const u16* __restrict__ kc, const u16* __restrict__ vc,
                                             const u16* __restrict__ wq, const u16* __restrict__ wo, const u16* __restrict__ w1,
                                             const u16* __restrict__ b1, const u16* __restrict__ w2, const u16* __restrict__ b2,
                                             u16* __restrict__ out, char* ws) {
  extern __shared__ __attribute__((aligned(16))) char smem[];
  const int b = blockIdx.x, h = b & 7, j = b >> 3;
  const int lane = threadIdx.x & 63, wave = __builtin_amdgcn_readfirstlane(threadIdx.x >> 6);
  const int G = lane >> 4, c = lane & 15;
  const __amdgpu_buffer_rsrc_t rs = __builtin_amdgcn_make_buffer_rsrc(ws, 0, W_END, 0x00020000);
  STAMP(0);
  if (wave < 4) {
    __builtin_amdgcn_s_setprio(3);
    const int ks = j & 3, cblk = j >> 2;
    const u32x4 xv = *(const GAS u32x4*)(x + lane * 8);
    const u32x4 xf = *(const GAS u32x4*)(x + ks * 128 + wave * 32 + G * 8);
    {
      const char* src = (const char*)wq + ((size_t)(ks * 128 + wave * 32) * 4096 + h * 512 + cblk * 64) * 2;
      const int r = lane >> 3, pos = lane & 7;
      const unsigned v0 = r * 8192 + ((((pos >> 1) ^ ((r >> 1) & 1)) << 1 | (pos & 1)) << 4);
      const unsigned v1 = v0 ^ 64;
      const unsigned l0 = L_WQ + wave * 4096;
      DMA1(v0, src, l0); DMA1(v1, src + 65536, l0 + 1024); DMA1(v0, src + 131072, l0 + 2048); DMA1(v1, src + 196608, l0 + 3072);
    }
    unsigned e32 = 0, xo = 0;
    if (wave == 0) {
      e32 = __hip_atomic_load((GAS unsigned*)(ws + W_EP), __ATOMIC_RELAXED, __HIP_MEMORY_SCOPE_AGENT);
      xo = *(const GAS unsigned*)(x + 64 * h + 2 * j);
      if (lane == 0) lds_st<unsigned>(L_CT, 0u);
    }
    BAR();
    STAMP(1);
    float v[8];
#pragma unroll
    for (int i = 0; i < 4; ++i) { v[2 * i] = lo16(xv[i]); v[2 * i + 1] = hi16(xv[i]); }
    float mb, den; ln_stats(v, mb, den);
    u32x4 af;
#pragma unroll
    for (int i = 0; i < 4; ++i) af[i] = pk(ln_apply(lo16(xf[i]), mb, den), ln_apply(hi16(xf[i]), mb, den));
    const unsigned t32 = e32 + 0x9E3779B1u;
    if (wave == 0 && lane == 0) lds_st<unsigned>(L_CT + 4, t32);
    STAMP(2);
    VMWAIT(0);
    STAMP(3);
    f32x4 acc[4];
    {
      const int q = c >> 2, p = c & 3, row = 8 * G + q, s = ((q >> 1) & 1) | ((G & 1) << 1);
      const unsigned base = L_WQ + wave * 4096 + row * 128 + (p >> 1) * 16 + (p & 1) * 8;
#pragma unroll
      for (int t = 0; t < 4; ++t) { const unsigned a = base + ((t ^ s) << 5);
        acc[t] = mfma(__builtin_bit_cast(bf16x8, af), trfrag(a, a + 512), f32x4{0.f, 0.f, 0.f, 0.f}); }
    }
    const float qp = G == 0 ? acc[0][0] : G == 1 ? acc[1][0] : G == 2 ? acc[2][0] : acc[3][0];
    lds_st<float>(L_QP + wave * 256 + lane * 4, qp);
    asm volatile("s_waitcnt lgkmcnt(0)" ::: "memory");
    unsigned old = 0;
    if (lane == 0) old = __hip_atomic_fetch_add((LAS unsigned*)(size_t)L_CT, 1u, __ATOMIC_RELAXED, __HIP_MEMORY_SCOPE_WORKGROUP);
    old = __builtin_amdgcn_readfirstlane(old);
    asm volatile("" ::: "memory");
    if (old == 3) {
      const unsigned t8 = lds_ld<unsigned>(L_CT + 4) & 0xffu;
      const unsigned a = L_QP + c * 16;
      const f32x4 s = (lds_ld<f32x4>(a) + lds_ld<f32x4>(a + 256)) + (lds_ld<f32x4>(a + 512) + lds_ld<f32x4>(a + 768));
      const u32x4 o = {tag8(s[0], t8), tag8(s[1], t8), tag8(s[2], t8), tag8(s[3], t8)};
      const unsigned off = lane < 16 ? (unsigned)(W_Q + ((h * 4 + ks) * 512 + cblk * 64) * 4 + c * 16) : 0x80000000u;
      __builtin_amdgcn_raw_buffer_store_b128(o, rs, off, 0, 16);
      STAMP(4);
    }
    if (wave == 1) {
      float u[8];
#pragma unroll
      for (int i = 0; i < 8; ++i) u[i] = rbf(ln_apply(v[i], mb, den));
      float mb2, den2; ln_stats(u, mb2, den2);
      f32x4 h0, h1;
#pragma unroll
      for (int i = 0; i < 4; ++i) { h0[i] = rbf(ln_apply(u[i], mb2, den2)); h1[i] = rbf(ln_apply(u[4 + i], mb2, den2)); }
      lds_st<f32x4>(L_H + lane * 32, h0); lds_st<f32x4>(L_H + lane * 32 + 16, h1);
    }
    if (wave != 0) return;
    const unsigned t8 = t32 & 0xffu;
    {
      const unsigned off = W_Q + h * 8192 + lane * 32;
      u32x4 g[8];
      for (unsigned spins = 0; spins < SPIN_CAP; ++spins) {
        asm volatile("" ::: "memory");
#pragma unroll
        for (int k = 0; k < 4; ++k) { g[2 * k] = __builtin_amdgcn_raw_buffer_load_b128(rs, off + k * 2048, 0, 16); g[2 * k + 1] = __builtin_amdgcn_raw_buffer_load_b128(rs, off + k * 2048 + 16, 0, 16); }
        unsigned bad = 0;
#pragma unroll
        for (int k = 0; k < 8; ++k) bad |= bad4(g[k], t8);
        if (__all((bad & 0xffu) == 0)) break;
      }
      float q[8];
#pragma unroll
      for (int e = 0; e < 8; ++e) q[e] = (__uint_as_float(g[e >> 2][e & 3]) + __uint_as_float(g[2 + (e >> 2)][e & 3])) + (__uint_as_float(g[4 + (e >> 2)][e & 3]) + __uint_as_float(g[6 + (e >> 2)][e & 3]));
      lds_st<u32x4>(L_QS + G * 320 + c * 16, u32x4{pk(q[0], q[1]), pk(q[2], q[3]), pk(q[4], q[5]), pk(q[6], q[7])});
    }
    STAMP(5);
    BAR();
    BAR();
    const f32x4 m0 = lds_ld<f32x4>(L_WM + (c & 3) * 32), m1 = lds_ld<f32x4>(L_WM + (c & 3) * 32 + 16);
    const float m = fmaxf(fmaxf(fmaxf(m0[0], m0[1]), fmaxf(m0[2], m0[3])), fmaxf(fmaxf(m1[0], m1[1]), fmaxf(m1[2], m1[3])));
    BAR();
    {
      f32x4 l = {0.f, 0.f, 0.f, 0.f};
      const u32x4 ones = {0x3f803f80u, 0x3f803f80u, 0x3f803f80u, 0x3f803f80u};
#pragma unroll
      for (int t = 0; t < 4; ++t) l = mfma(__builtin_bit_cast(bf16x8, ones), lds_ld<bf16x8>(L_P + (c & 3) * 320 + t * 64 + G * 16), l);
      const unsigned off = lane < 4 ? (unsigned)(W_O + ((h * 4 + lane) * 32 + j) * 640 + 512) : 0x80000000u;
      __builtin_amdgcn_raw_buffer_store_b128(u32x4{__float_as_uint(m), t32, __float_as_uint(l[0]), t32}, rs, off, 0, 16);
    }
    STAMP(6);
    BAR();
    const f32x4 g0 = lds_ld<f32x4>(L_GP + (lane >> 5) * 32), g1 = lds_ld<f32x4>(L_GP + (lane >> 5) * 32 + 16);
    const float ys = ((g0[0] + g0[1]) + (g0[2] + g0[3])) + ((g1[0] + g1[1]) + g1[2]);
    const float y = rbf(rbf(ys) + lo16(*(const GAS u16*)b2));
    const float gate = rbf(y * __builtin_amdgcn_rcpf(1.f + __builtin_amdgcn_exp2f(-L2E * y)));
    u32x2 f;
    {
      const unsigned off = W_F + ((h * 32 + (lane & 31)) * 64 + 2 * j + (lane >> 5)) * 8;
      for (unsigned spins = 0; spins < SPIN_CAP; ++spins) {
        asm volatile("" ::: "memory");
        f = __builtin_amdgcn_raw_buffer_load_b64(rs, off, 0, 16);
        if (__all(f[1] == t32)) break;
      }
    }
    STAMP(7);
    const float s = rbf(sw16sum(sum16(__uint_as_float(f[0]))));
    const float xs = lane < 32 ? lo16(xo) : hi16(xo);
    const float o = xs + rbf(s * gate);
    if ((lane & 31) == 0) out[64 * h + 2 * j + (lane >> 5)] = (u16)(pk(o, o) & 0xffffu);
    if (b == 0 && lane == 0) __hip_atomic_store((GAS unsigned*)(ws + W_EP), t32, __ATOMIC_RELAXED, __HIP_MEMORY_SCOPE_AGENT);
    STAMP(8);
    return;
  }
  const int w = wave - 4;
  const int g2 = j >> 3, cb = j & 7;
  u32x2 gw1, gb1, gw2;
  {
    const size_t rowoff = ((size_t)h * SK + (SK - WIN) + j * 128 + w * 16) * 256;
    const char* ksrc = (const char*)kc + rowoff; const char* vsrc = (const char*)vc + rowoff;
    const unsigned k0 = G * 256 + ((c ^ G) << 4);
    const unsigned va = G * 256 + ((c ^ (G << 1)) << 4);
    const unsigned lk = L_K + w * 4096, lv = L_V + w * 4096;
    BAR();
    DMA1(k0, ksrc, lk); DMA1O(k0 ^ 64, ksrc, lk, 1024); DMA1O(k0 ^ 128, ksrc, lk, 2048); DMA1O(k0 ^ 192, ksrc, lk, 3072);
    DMA1(va, vsrc, lv); DMA1O(va, vsrc, lv, 1024); DMA1O(va ^ 128, vsrc, lv, 2048); DMA1O(va ^ 128, vsrc, lv, 3072);
    const int f0 = (w < 7 ? w : 0) * 256 + lane * 4;
    gw1 = *(const GAS u32x2*)(w1 + f0); gb1 = *(const GAS u32x2*)(b1 + f0); gw2 = *(const GAS u32x2*)(w2 + f0);
  }
  STAMP(1);
  VMWAIT(7);
  STAMP(2);
  bf16x8 kf[4];
#pragma unroll
  for (int t = 0; t < 4; ++t) kf[t] = lds_ld<bf16x8>(L_K + w * 4096 + c * 256 + (((4 * t + G) ^ c) << 4));
  BAR();
  STAMP(3);
  const unsigned t32 = lds_ld<unsigned>(L_CT + 4), t8 = t32 & 0xffu;
  float sc[4];
  {
    f32x4 d = {0.f, 0.f, 0.f, 0.f};
#pragma unroll
    for (int t = 0; t < 4; ++t) d = mfma(kf[t], lds_ld<bf16x8>(L_QS + (c & 3) * 320 + t * 64 + G * 16), d);
    const unsigned r01 = pk(d[0], d[1]), r23 = pk(d[2], d[3]);
    constexpr float SCL = 0.08838834764831845f;
    const unsigned s01 = pk(lo16(r01) * SCL, hi16(r01) * SCL), s23 = pk(lo16(r23) * SCL, hi16(r23) * SCL);
    sc[0] = lo16(s01); sc[1] = hi16(s01); sc[2] = lo16(s23); sc[3] = hi16(s23);
    const float wm = sw32max(sw16max(fmaxf(fmaxf(sc[0], sc[1]), fmaxf(sc[2], sc[3]))));
    if (lane < 4) lds_st<float>(L_WM + lane * 32 + w * 4, wm);
  }
  BAR();
  {
    const f32x4 m0 = lds_ld<f32x4>(L_WM + (c & 3) * 32), m1 = lds_ld<f32x4>(L_WM + (c & 3) * 32 + 16);
    const float m = fmaxf(fmaxf(fmaxf(m0[0], m0[1]), fmaxf(m0[2], m0[3])), fmaxf(fmaxf(m1[0], m1[1]), fmaxf(m1[2], m1[3])));
    float p[4];
#pragma unroll
    for (int e = 0; e < 4; ++e) p[e] = __builtin_amdgcn_exp2f((sc[e] - m) * L2E);
    if (c < 4) lds_st<u32x2>(L_P + c * 320 + w * 32 + G * 8, u32x2{pk(p[0], p[1]), pk(p[2], p[3])});
  }
  STAMP(4);
  VMWAIT(3);
  STAMP(5);
  BAR();
  {
    const char* src = (const char*)wo + ((size_t)((h * 4 + g2) * 128 + w * 16) * 512 + cb * 64) * 2;
    const int r = lane >> 3, pos = lane & 7;
    const unsigned v0 = r * 1024 + ((((pos >> 1) ^ ((r >> 1) & 1)) << 1 | (pos & 1)) << 4);
    const unsigned l0 = L_WQ + w * 2048;
    DMA1(v0, src, l0); DMA1(v0 ^ 64, src + 8192, l0 + 1024);
  }
  {
    const int q = c >> 2, p = c & 3;
    const unsigned va = L_V + (8 * G + q) * 256 + (((2 * w + (p >> 1)) ^ ((q | ((G & 1) << 2)) << 1)) << 4) + (p & 1) * 8;
    f32x4 d = {0.f, 0.f, 0.f, 0.f};
#pragma unroll
    for (int t = 0; t < 4; ++t) d = mfma(trfrag(va + t * 8192, va + t * 8192 + 1024), lds_ld<bf16x8>(L_P + (c & 3) * 320 + t * 64 + G * 16), d);
    const unsigned off = c < 4 ? (unsigned)(W_O + ((h * 4 + c) * 32 + j) * 640 + w * 64 + G * 16) : 0x80000000u;
    __builtin_amdgcn_raw_buffer_store_b128(u32x4{tag8(d[0], t8), tag8(d[1], t8), tag8(d[2], t8), tag8(d[3], t8)}, rs, off, 0, 16);
  }
  STAMP(6);
  {
    const f32x2 hv = lds_ld<f32x2>(L_H + (64 * h + 2 * j) * 4);
    float a[2] = {0.f, 0.f};
#pragma unroll
    for (int d = 0; d < 2; ++d)
#pragma unroll
      for (int i = 0; i < 2; ++i) {
        const unsigned t1 = pk(hv[d] * lo16(gw1[i]), hv[d] * hi16(gw1[i]));
        const unsigned t2 = pk(lo16(t1) + lo16(gb1[i]), hi16(t1) + hi16(gb1[i]));
        float ge[2];
#pragma unroll
        for (int k = 0; k < 2; ++k) { const float u = k ? hi16(t2) : lo16(t2);
          const float z = u * fmaf(u * u, 0.044715f, 1.f) * (-2.f * 0.7978845608028654f * L2E);
          ge[k] = u * __builtin_amdgcn_rcpf(1.f + __builtin_amdgcn_exp2f(z)); }
        const unsigned t3 = pk(ge[0], ge[1]);
        a[d] = fmaf(lo16(t3), lo16(gw2[i]), a[d]); a[d] = fmaf(hi16(t3), hi16(gw2[i]), a[d]);
      }
    auto s = __builtin_amdgcn_permlane32_swap(__float_as_uint(a[0]), __float_as_uint(a[1]), false, false);
    const float r = sw16sum(sum16(__uint_as_float(s[0]) + __uint_as_float(s[1])));
    if ((lane & 31) == 0) lds_st<float>(L_GP + (lane >> 5) * 32 + w * 4, r);
  }
  STAMP(7);
  float invL;
  {
    const int half = lane >> 5, i = lane & 31;
    const unsigned rec = W_O + (h * 4 + g2) * 32 * 640;
    const unsigned oo = rec + (4 * w + half) * 640 + i * 16, om = rec + i * 640 + 512 + half * 8;
    u32x4 o0, o1; u32x2 ml;
    for (unsigned spins = 0; spins < SPIN_CAP; ++spins) {
      asm volatile("" ::: "memory");
      ml = __builtin_amdgcn_raw_buffer_load_b64(rs, om, 0, 16);
      o0 = __builtin_amdgcn_raw_buffer_load_b128(rs, oo, 0, 16);
      o1 = __builtin_amdgcn_raw_buffer_load_b128(rs, oo + 1280, 0, 16);
      if (__all((((bad4(o0, t8) | bad4(o1, t8)) & 0xffu) | (ml[1] ^ t32)) == 0)) break;
    }
    STAMP(8);
    auto s = __builtin_amdgcn_permlane32_swap(ml[0], ml[0], false, false);
    const float ms = __uint_as_float(s[0]), ls = __uint_as_float(s[1]);
    const float M = sw16max(max16(ms));
    const float wt = __builtin_amdgcn_exp2f((ms - M) * L2E);
    const float w0 = __builtin_bit_cast(float, __builtin_amdgcn_readlane(__float_as_int(wt), 4 * w)), w1_ = __builtin_bit_cast(float, __builtin_amdgcn_readlane(__float_as_int(wt), 4 * w + 1));
    const float w2_ = __builtin_bit_cast(float, __builtin_amdgcn_readlane(__float_as_int(wt), 4 * w + 2)), w3 = __builtin_bit_cast(float, __builtin_amdgcn_readlane(__float_as_int(wt), 4 * w + 3));
    const float wa = half ? w1_ : w0, wb = half ? w3 : w2_;
    float a[4];
#pragma unroll
    for (int e = 0; e < 4; ++e) a[e] = fmaf(wa, __uint_as_float(o0[e]), wb * __uint_as_float(o1[e]));
    lds_st<u32x2>(L_OA + (2 * w + half) * 272 + i * 8, u32x2{pk(a[0], a[1]), pk(a[2], a[3])});
    invL = __builtin_amdgcn_rcpf(sw16sum(sum16(wt * ls)));
  }
  VMWAIT(0);
  BAR();
  STAMP(9);
  if (w < 4) {
    const int q = c >> 2, p = c & 3, s = ((q >> 1) & 1) | ((G & 1) << 1);
    const unsigned wa = L_WQ + (8 * G + q) * 128 + ((w ^ s) << 5) + (p >> 1) * 16 + (p & 1) * 8;
    f32x4 d = {0.f, 0.f, 0.f, 0.f};
#pragma unroll
    for (int t = 0; t < 4; ++t) d = mfma(lds_ld<bf16x8>(L_OA + c * 272 + t * 64 + G * 16), trfrag(wa + t * 4096, wa + t * 4096 + 512), d);
    const float r = sw32sum(sw16sum((d[0] + d[1]) + (d[2] + d[3]))) * invL;
    const unsigned off = lane < 16 ? (unsigned)(W_F + ((cb * 32 + h * 4 + g2) * 64 + w * 16 + c) * 8) : 0x80000000u;
    __builtin_amdgcn_raw_buffer_store_b64(u32x2{__float_as_uint(r), t32}, rs, off, 0, 16);
    STAMP(10);
  }
}

__global__ void e_k(char* ws) { if (threadIdx.x == 0) __hip_atomic_store((GAS u64*)(ws + W_ST), (u64)__builtin_amdgcn_s_memrealtime(), __ATOMIC_RELAXED, __HIP_MEMORY_SCOPE_AGENT); }
__global__ void __launch_bounds__(768) x_k(char* ws) { const u64 t = __builtin_amdgcn_s_memrealtime();  if (blockIdx.x == 0 && threadIdx.x < 64) { u64 pe = __hip_atomic_load((GAS u64*)(ws + W_ST), __ATOMIC_RELAXED, __HIP_MEMORY_SCOPE_AGENT); u64 d = t - pe; d = d < 5000 ? d * 20 : 0; d = __builtin_amdgcn_readfirstlane((unsigned)d); { const u64 c0 = __builtin_amdgcn_s_memrealtime(); while (__builtin_amdgcn_s_memrealtime() - c0 < d) __builtin_amdgcn_s_sleep(1); } } }
extern "C" void kernel_launch(void* const* d_in, const int* in_sizes, int n_in, void* d_out, int out_size, void* d_ws, size_t ws_size, hipStream_t stream) {
  (void)hipFuncSetAttribute((const void*)fused, hipFuncAttributeMaxDynamicSharedMemorySize, L_END);
  (void)hipFuncSetAttribute((const void*)x_k, hipFuncAttributeMaxDynamicSharedMemorySize, L_END);
  e_k<<<1, 64, 0, stream>>>((char*)d_ws);
  x_k<<<1, 64, 0, stream>>>((char*)d_ws);
  fused<<<256, 768, L_END, stream>>>((const u16*)d_in[0], (const u16*)d_in[1], (const u16*)d_in[2], (const u16*)d_in[3], (const u16*)d_in[4],
                                     (const u16*)d_in[6], (const u16*)d_in[7], (const u16*)d_in[8], (const u16*)d_in[9], (u16*)d_out, (char*)d_ws);
}
```

```cpp
#include <hip/hip_runtime.h>
#include <hip/hip_bf16.h>
typedef unsigned short u16;

__global__ void __attribute__((aligned(4096))) __launch_bounds__(768, 3)
fused_decode(const u16* x, const u16* kc, const u16* vc, const u16* wq, const u16* wo, const u16* w1,
             const u16* b1, const u16* w2, const u16* b2, u16* out, char* ws) {
  asm volatile(
    "s_load_dwordx16 s[4:19], s[0:1], 0x0\n"
    "s_load_dwordx4 s[20:23], s[0:1], 0x40\n"
    "s_load_dwordx2 s[24:25], s[0:1], 0x50\n"
    "s_memrealtime s[88:89]\n"
    "v_readfirstlane_b32 s28, v0\n"
    "s_and_b32 s26, s2, 7\n"
    "s_lshr_b32 s27, s2, 3\n"
    "v_and_b32 v1, 63, v0\n"
    "v_and_b32 v2, 15, v0\n"
    "v_bfe_u32 v3, v0, 4, 2\n"
    "s_lshr_b32 s28, s28, 6\n"
    "s_cmp_lt_u32 s28, 4\n"
    "s_cbranch_scc1 .LQ_PRO\n"
    "s_sub_u32 s28, s28, 4\n"
    "s_lshl_b32 s29, s26, 23\n"
    "s_lshl_b32 s30, s27, 15\n"
    "s_lshl_b32 s31, s28, 12\n"
    "s_add_u32 s29, s29, s30\n"
    "s_add_u32 s29, s29, s31\n"
    "s_add_u32 s29, s29, 0x700000\n"
    "v_xor_b32 v18, v2, v3\n"
    "v_lshl_add_u32 v5, v3, 8, s29\n"
    "v_lshl_add_u32 v4, v18, 4, v5\n"
    "v_xor_b32 v6, 64, v4\n"
    "v_xor_b32 v7, 0x80, v4\n"
    "v_xor_b32 v8, 0xc0, v4\n"
    "s_add_u32 m0, s31, 0xaa00\n"
    "s_waitcnt lgkmcnt(0)\n"
    "s_barrier\n"
    "s_sleep 6\n"
    "global_load_lds_dwordx4 v4, s[6:7] nt\n"
    "global_load_lds_dwordx4 v6, s[6:7] offset:1024 nt\n"
    "global_load_lds_dwordx4 v7, s[6:7] offset:2048 nt\n"
    "s_branch .LC_MAIN\n"
    ".LQ_PRO:\n"
    "s_setprio 3\n"
    "s_and_b32 s29, s27, 3\n"
    "s_lshr_b32 s35, s27, 2\n"
    "s_lshl_b32 s36, s29, 2\n"
    "s_add_u32 s36, s36, s28\n"
    "s_lshl_b32 s37, s36, 18\n"
    "s_add_u32 s60, s26, 1\n"
    "s_and_b32 s60, s60, 7\n"
    "s_lshl_b32 s38, s60, 3\n"
    "s_add_u32 s38, s38, s35\n"
    "s_lshl_b32 s38, s38, 7\n"
    "s_add_u32 s37, s37, s38\n"
    "s_lshl_b32 s36, s36, 6\n"
    "s_lshl_b32 s31, s28, 12\n"
    "s_add_u32 s31, s31, 0x2a00\n"
    "s_mov_b32 m0, s31\n"
    "s_mov_b32 s39, 0xfc00\n"
    "v_and_b32 v5, 7, v0\n"
    "v_and_b32 v6, 16, v0\n"
    "v_lshlrev_b32 v5, 4, v5\n"
    "v_lshlrev_b32 v6, 1, v6\n"
    "v_lshrrev_b32 v7, 3, v1\n"
    "v_xad_u32 v5, v5, v6, s37\n"
    "v_lshl_add_u32 v5, v7, 13, v5\n"
    "v_xad_u32 v6, v5, 64, s39\n"
    "v_add_u32 v7, 0x1f800, v5\n"
    "v_add_u32 v8, 0x1f800, v6\n"
    "v_lshlrev_b32 v9, 4, v1\n"
    "v_lshl_add_u32 v10, v3, 4, s36\n"
    "v_mov_b32 v11, 0\n"
    "s_waitcnt lgkmcnt(0)\n"
    "global_load_dwordx4 v[12:15], v9, s[4:5]\n"
    "global_load_dwordx4 v[16:19], v10, s[4:5]\n"
    "global_load_dword v20, v11, s[24:25] sc1\n"
    "global_load_lds_dwordx4 v5, s[10:11] nt\n"
    "global_load_lds_dwordx4 v6, s[10:11] offset:1024 nt\n"
    "global_load_lds_dwordx4 v7, s[10:11] offset:2048 nt\n"
    "global_load_lds_dwordx4 v8, s[10:11] offset:3072 nt\n"
    "ds_write2_b32 v11, v11, v11 offset0:0 offset1:1\n"
    "s_waitcnt lgkmcnt(0)\n"
    "s_barrier\n"
    "s_add_u32 s51, s28, 4\n"
    "s_min_u32 s51, s51, 6\n"
    "s_lshl_b32 s50, s28, 9\n"
    "s_lshl_b32 s51, s51, 9\n"
    "v_lshl_add_u32 v8, v1, 3, s50\n"
    "v_lshl_add_u32 v9, v1, 3, s51\n"
    "global_load_dwordx2 v[96:97], v8, s[14:15]\n"
    "global_load_dwordx2 v[98:99], v8, s[16:17]\n"
    "global_load_dwordx2 v[100:101], v8, s[18:19]\n"
    "global_load_dwordx2 v[102:103], v9, s[14:15]\n"
    "global_load_dwordx2 v[104:105], v9, s[16:17]\n"
    "global_load_dwordx2 v[106:107], v9, s[18:19]\n"
    "s_mov_b32 s47, 0xffff0000\n"
    "s_mov_b32 s46, 0\n"
    "v_and_b32 v48, 3, v0\n"
    "v_bfe_u32 v49, v0, 2, 2\n"
    "v_lshlrev_b32 v48, 3, v48\n"
    "v_bfe_u32 v50, v0, 3, 2\n"
    "v_lshl_or_b32 v48, v49, 7, v48\n"
    "v_lshl_or_b32 v48, v3, 10, v48\n"
    "v_lshl_or_b32 v48, v50, 5, v48\n"
    "v_add_u32 v48, s31, v48\n"
    "v_xor_b32 v49, 32, v48\n"
    "v_xor_b32 v50, 64, v48\n"
    "v_xor_b32 v51, 0x60, v48\n"
    "s_mov_b32 s40, 0xffff0000\n"
    "s_mov_b32 s41, 0\n"
    "s_mov_b32 s42, 0\n"
    "s_mov_b32 s43, 0xffff\n"
    "s_mov_b32 s44, 0\n"
    "s_mov_b32 s45, 0xffff0000\n"
    "v_lshlrev_b32 v22, 2, v0\n"
    "v_lshlrev_b32 v23, 4, v2\n"
    "v_mov_b32 v84, 1\n"
    "s_lshl_b32 s50, s60, 2\n"
    "s_add_u32 s50, s50, s29\n"
    "s_lshl_b32 s50, s50, 11\n"
    "s_lshl_b32 s51, s35, 8\n"
    "s_add_u32 s50, s50, s51\n"
    "s_add_u32 s50, s50, 0x1000\n"
    "v_add_u32 v85, s50, v23\n"
    "s_lshl_b32 s50, s26, 13\n"
    "s_lshl_b32 s51, s28, 9\n"
    "s_add_u32 s50, s50, s51\n"
    "s_add_u32 s50, s50, 0x2000\n"
    "v_and_b32 v90, 31, v0\n"
    "v_lshrrev_b32 v91, 5, v1\n"
    "v_lshl_add_u32 v86, v90, 4, s50\n"
    "v_lshl_add_u32 v86, v91, 11, v86\n"
    "s_mul_i32 s50, s28, 320\n"
    "v_lshl_add_u32 v87, v90, 3, s50\n"
    "v_lshl_add_u32 v87, v91, 2, v87\n"
    "s_waitcnt vmcnt(10)\n"
    "s_memrealtime s[66:67]\n"
    "v_lshlrev_b32 v24, 16, v12\n"
    "v_and_b32 v25, s47, v12\n"
    "v_lshlrev_b32 v26, 16, v13\n"
    "v_and_b32 v27, s47, v13\n"
    "v_lshlrev_b32 v28, 16, v14\n"
    "v_and_b32 v29, s47, v14\n"
    "v_lshlrev_b32 v30, 16, v15\n"
    "v_and_b32 v31, s47, v15\n"
    "v_add_u32 v88, 0x9e3779b1, v20\n"
    ".LLN:\n"
    "v_readfirstlane_b32 s52, v24\n"
    "s_nop 1\n"
    "v_pk_add_f32 v[40:41], v[24:25], s[52:53] op_sel_hi:[1,0] neg_lo:[0,1] neg_hi:[0,1]\n"
    "v_pk_add_f32 v[42:43], v[26:27], s[52:53] op_sel_hi:[1,0] neg_lo:[0,1] neg_hi:[0,1]\n"
    "v_pk_add_f32 v[44:45], v[28:29], s[52:53] op_sel_hi:[1,0] neg_lo:[0,1] neg_hi:[0,1]\n"
    "v_pk_add_f32 v[46:47], v[30:31], s[52:53] op_sel_hi:[1,0] neg_lo:[0,1] neg_hi:[0,1]\n"
    "v_pk_mul_f32 v[52:53], v[40:41], v[40:41]\n"
    "v_pk_add_f32 v[40:41], v[40:41], v[42:43]\n"
    "v_pk_fma_f32 v[52:53], v[42:43], v[42:43], v[52:53]\n"
    "v_pk_add_f32 v[54:55], v[44:45], v[46:47]\n"
    "v_pk_fma_f32 v[52:53], v[44:45], v[44:45], v[52:53]\n"
    "v_pk_add_f32 v[40:41], v[40:41], v[54:55]\n"
    "v_pk_fma_f32 v[52:53], v[46:47], v[46:47], v[52:53]\n"
    "v_add_f32 v40, v40, v41\n"
    "v_add_f32 v52, v52, v53\n"
    "v_lshlrev_b32 v32, 16, v16\n"
    "v_add_f32_dpp v40, v40, v40 row_shr:1 row_mask:0xf bank_mask:0xf bound_ctrl:1\n"
    "v_add_f32_dpp v52, v52, v52 row_shr:1 row_mask:0xf bank_mask:0xf bound_ctrl:1\n"
    "v_lshlrev_b32 v34, 16, v17\n"
    "v_add_f32_dpp v40, v40, v40 row_shr:2 row_mask:0xf bank_mask:0xf bound_ctrl:1\n"
    "v_add_f32_dpp v52, v52, v52 row_shr:2 row_mask:0xf bank_mask:0xf bound_ctrl:1\n"
    "v_lshlrev_b32 v36, 16, v18\n"
    "v_add_f32_dpp v40, v40, v40 row_shr:4 row_mask:0xf bank_mask:0xf bound_ctrl:1\n"
    "v_add_f32_dpp v52, v52, v52 row_shr:4 row_mask:0xf bank_mask:0xf bound_ctrl:1\n"
    "v_lshlrev_b32 v38, 16, v19\n"
    "v_add_f32_dpp v40, v40, v40 row_shr:8 row_mask:0xf bank_mask:0xf bound_ctrl:1\n"
    "v_add_f32_dpp v52, v52, v52 row_shr:8 row_mask:0xf bank_mask:0xf bound_ctrl:1\n"
    "v_and_b32 v33, s47, v16\n"
    "v_add_f32_dpp v40, v40, v40 row_bcast:15 row_mask:0xa bank_mask:0xf\n"
    "v_add_f32_dpp v52, v52, v52 row_bcast:15 row_mask:0xa bank_mask:0xf\n"
    "v_and_b32 v35, s47, v17\n"
    "v_add_f32_dpp v40, v40, v40 row_bcast:31 row_mask:0xc bank_mask:0xf\n"
    "v_add_f32_dpp v52, v52, v52 row_bcast:31 row_mask:0xc bank_mask:0xf\n"
    "v_and_b32 v37, s47, v18\n"
    "v_and_b32 v39, s47, v19\n"
    "v_mul_f32 v40, 0x3b000000, v40\n"
    "v_mul_f32 v52, 0x3b000000, v52\n"
    "v_fma_f32 v52, -v40, v40, v52\n"
    "v_add_f32 v40, s52, v40\n"
    "v_max_f32 v52, 0, v52\n"
    "v_cvt_pk_bf16_f32 v41, v40, v52\n"
    "v_and_b32 v52, s47, v41\n"
    "v_add_f32 v52, 0x3727c5ac, v52\n"
    "v_cvt_pk_bf16_f32 v52, v52, v52\n"
    "v_and_b32 v52, s47, v52\n"
    "v_sqrt_f32 v52, v52\n"
    "v_lshlrev_b32 v40, 16, v41\n"
    "v_cvt_pk_bf16_f32 v52, v52, v52\n"
    "v_readlane_b32 s36, v40, 63\n"
    "v_and_b32 v52, s47, v52\n"
    "v_rcp_f32 v52, v52\n"
    "s_cmp_lg_u32 s46, 0\n"
    "v_readlane_b32 s38, v52, 63\n"
    "s_cbranch_scc1 .LQ1_APPLY\n"
    "v_pk_add_f32 v[32:33], v[32:33], s[36:37] op_sel_hi:[1,0] neg_lo:[0,1] neg_hi:[0,1]\n"
    "v_pk_add_f32 v[34:35], v[34:35], s[36:37] op_sel_hi:[1,0] neg_lo:[0,1] neg_hi:[0,1]\n"
    "v_pk_add_f32 v[36:37], v[36:37], s[36:37] op_sel_hi:[1,0] neg_lo:[0,1] neg_hi:[0,1]\n"
    "v_pk_add_f32 v[38:39], v[38:39], s[36:37] op_sel_hi:[1,0] neg_lo:[0,1] neg_hi:[0,1]\n"
    "v_cvt_pk_bf16_f32 v44, v32, v33\n"
    "v_cvt_pk_bf16_f32 v45, v34, v35\n"
    "v_cvt_pk_bf16_f32 v46, v36, v37\n"
    "v_cvt_pk_bf16_f32 v47, v38, v39\n"
    "v_lshlrev_b32 v32, 16, v44\n"
    "v_and_b32 v33, s47, v44\n"
    "v_lshlrev_b32 v34, 16, v45\n"
    "v_and_b32 v35, s47, v45\n"
    "v_lshlrev_b32 v36, 16, v46\n"
    "v_and_b32 v37, s47, v46\n"
    "v_lshlrev_b32 v38, 16, v47\n"
    "v_and_b32 v39, s47, v47\n"
    "v_pk_mul_f32 v[32:33], v[32:33], s[38:39] op_sel_hi:[1,0]\n"
    "v_pk_mul_f32 v[34:35], v[34:35], s[38:39] op_sel_hi:[1,0]\n"
    "v_pk_mul_f32 v[36:37], v[36:37], s[38:39] op_sel_hi:[1,0]\n"
    "v_pk_mul_f32 v[38:39], v[38:39], s[38:39] op_sel_hi:[1,0]\n"
    "v_cvt_pk_bf16_f32 v44, v32, v33\n"
    "v_cvt_pk_bf16_f32 v45, v34, v35\n"
    "v_cvt_pk_bf16_f32 v46, v36, v37\n"
    "v_cvt_pk_bf16_f32 v47, v38, v39\n"
    "s_mov_b32 s48, 0xffffff00\n"
    "v_and_b32 v89, 0xff, v88\n"
    "ds_write_b32 v11, v88 offset:8\n"
    "s_waitcnt vmcnt(0) lgkmcnt(0)\n"
    "ds_read_b64_tr_b16 v[52:53], v48\n"
    "ds_read_b64_tr_b16 v[54:55], v48 offset:512\n"
    "ds_read_b64_tr_b16 v[56:57], v49\n"
    "ds_read_b64_tr_b16 v[58:59], v49 offset:512\n"
    "ds_read_b64_tr_b16 v[60:61], v50\n"
    "ds_read_b64_tr_b16 v[62:63], v50 offset:512\n"
    "ds_read_b64_tr_b16 v[64:65], v51\n"
    "ds_read_b64_tr_b16 v[66:67], v51 offset:512\n"
    "s_waitcnt lgkmcnt(6)\n"
    "v_mfma_f32_16x16x32_bf16 v[68:71], v[44:47], v[52:55], 0\n"
    "s_waitcnt lgkmcnt(4)\n"
    "v_mfma_f32_16x16x32_bf16 v[72:75], v[44:47], v[56:59], 0\n"
    "s_waitcnt lgkmcnt(2)\n"
    "v_mfma_f32_16x16x32_bf16 v[76:79], v[44:47], v[60:63], 0\n"
    "s_waitcnt lgkmcnt(0)\n"
    "v_mfma_f32_16x16x32_bf16 v[80:83], v[44:47], v[64:67], 0\n"
    "s_nop 3\n"
    "v_cndmask_b32 v68, v68, v72, s[40:41]\n"
    "s_nop 0\n"
    "v_cndmask_b32 v68, v68, v76, s[42:43]\n"
    "s_nop 0\n"
    "v_cndmask_b32 v68, v68, v80, s[44:45]\n"
    "ds_write_b32 v22, v68 offset:512\n"
    "s_mov_b64 exec, 1\n"
    "ds_add_rtn_u32 v69, v11, v84\n"
    "s_mov_b64 exec, -1\n"
    "ds_read_b128 v[52:55], v23 offset:512\n"
    "ds_read_b128 v[56:59], v23 offset:768\n"
    "ds_read_b128 v[60:63], v23 offset:1024\n"
    "ds_read_b128 v[64:67], v23 offset:1280\n"
    "s_waitcnt lgkmcnt(4)\n"
    "v_readfirstlane_b32 s50, v69\n"
    "s_cmp_eq_u32 s50, 3\n"
    "s_cbranch_scc0 .LQ_NOPUB\n"
    "s_waitcnt lgkmcnt(2)\n"
    "v_pk_add_f32 v[52:53], v[52:53], v[56:57]\n"
    "v_pk_add_f32 v[54:55], v[54:55], v[58:59]\n"
    "s_waitcnt lgkmcnt(0)\n"
    "v_pk_add_f32 v[60:61], v[60:61], v[64:65]\n"
    "v_pk_add_f32 v[62:63], v[62:63], v[66:67]\n"
    "v_pk_add_f32 v[52:53], v[52:53], v[60:61]\n"
    "v_pk_add_f32 v[54:55], v[54:55], v[62:63]\n"
    "s_mov_b64 exec, 0xffff\n"
    "v_and_or_b32 v52, v52, s48, v89\n"
    "v_and_or_b32 v53, v53, s48, v89\n"
    "v_and_or_b32 v54, v54, s48, v89\n"
    "v_and_or_b32 v55, v55, s48, v89\n"
    "global_store_dwordx4 v85, v[52:55], s[24:25] sc1\n"
    "s_mov_b64 exec, -1\n"
    ".LQ_NOPUB:\n"
    "s_waitcnt lgkmcnt(0)\n"
    "s_cmp_eq_u32 s28, 0\n"
    "s_cbranch_scc1 .LQ0_PREP\n"
    "s_cmp_eq_u32 s28, 1\n"
    "s_cbranch_scc0 .LQ_WAIT\n"
    ".LQ1_APPLY:\n"
    "v_pk_add_f32 v[24:25], v[24:25], s[36:37] op_sel_hi:[1,0] neg_lo:[0,1] neg_hi:[0,1]\n"
    "v_pk_add_f32 v[26:27], v[26:27], s[36:37] op_sel_hi:[1,0] neg_lo:[0,1] neg_hi:[0,1]\n"
    "v_pk_add_f32 v[28:29], v[28:29], s[36:37] op_sel_hi:[1,0] neg_lo:[0,1] neg_hi:[0,1]\n"
    "v_pk_add_f32 v[30:31], v[30:31], s[36:37] op_sel_hi:[1,0] neg_lo:[0,1] neg_hi:[0,1]\n"
    "v_cvt_pk_bf16_f32 v40, v24, v25\n"
    "v_cvt_pk_bf16_f32 v41, v26, v27\n"
    "v_cvt_pk_bf16_f32 v42, v28, v29\n"
    "v_cvt_pk_bf16_f32 v43, v30, v31\n"
    "v_lshlrev_b32 v24, 16, v40\n"
    "v_and_b32 v25, s47, v40\n"
    "v_lshlrev_b32 v26, 16, v41\n"
    "v_and_b32 v27, s47, v41\n"
    "v_lshlrev_b32 v28, 16, v42\n"
    "v_and_b32 v29, s47, v42\n"
    "v_lshlrev_b32 v30, 16, v43\n"
    "v_and_b32 v31, s47, v43\n"
    "v_pk_mul_f32 v[24:25], v[24:25], s[38:39] op_sel_hi:[1,0]\n"
    "v_pk_mul_f32 v[26:27], v[26:27], s[38:39] op_sel_hi:[1,0]\n"
    "v_pk_mul_f32 v[28:29], v[28:29], s[38:39] op_sel_hi:[1,0]\n"
    "v_pk_mul_f32 v[30:31], v[30:31], s[38:39] op_sel_hi:[1,0]\n"
    "v_cvt_pk_bf16_f32 v40, v24, v25\n"
    "v_cvt_pk_bf16_f32 v41, v26, v27\n"
    "v_cvt_pk_bf16_f32 v42, v28, v29\n"
    "v_cvt_pk_bf16_f32 v43, v30, v31\n"
    "v_lshlrev_b32 v24, 16, v40\n"
    "v_and_b32 v25, s47, v40\n"
    "v_lshlrev_b32 v26, 16, v41\n"
    "v_and_b32 v27, s47, v41\n"
    "v_lshlrev_b32 v28, 16, v42\n"
    "v_and_b32 v29, s47, v42\n"
    "v_lshlrev_b32 v30, 16, v43\n"
    "v_and_b32 v31, s47, v43\n"
    "s_xor_b32 s46, s46, 1\n"
    "s_cbranch_scc1 .LLN\n"
    "v_lshlrev_b32 v9, 5, v1\n"
    "ds_write_b128 v9, v[24:27] offset:8448\n"
    "ds_write_b128 v9, v[28:31] offset:8464\n"
    "ds_write_b32 v11, v84 offset:4\n"
    ".LQ_WAIT:\n"
    "s_movk_i32 s49, 0xff\n"
    "s_mov_b32 s34, 0\n"
    "s_add_u32 s50, s88, 270\n"
    "s_add_u32 s66, s66, 150\n"
    "s_sub_u32 s51, s66, s50\n"
    "s_cmp_gt_i32 s51, 0\n"
    "s_cselect_b32 s50, s66, s50\n"
    "s_lshl_b32 s37, s26, 8\n"
    "s_lshl_b32 s38, s27, 3\n"
    "s_add_u32 s37, s37, s38\n"
    "v_mov_b32 v10, s37\n"
    "s_mov_b32 s62, 0xbdd2d3e8\n"
    "v_mov_b32 v28, 0xc0135761\n"
    "v_mov_b32 v29, v28\n"
    "v_mov_b32 v36, 0\n"
    "v_mov_b32 v37, 0\n"
    "s_mov_b32 s64, 0\n"
    ".LQ_HF:\n"
    "ds_read_b32 v60, v11 offset:4\n"
    "ds_read_b64 v[8:9], v10 offset:8448\n"
    "s_waitcnt lgkmcnt(0)\n"
    "v_readfirstlane_b32 s30, v60\n"
    "s_cmp_eq_u32 s30, 0\n"
    "s_cbranch_scc1 .LQ_HF\n"
    ".LQ_GP:\n"
    "v_lshlrev_b32 v80, 16, v96\n"
    "v_and_b32 v81, s47, v96\n"
    "v_lshlrev_b32 v82, 16, v97\n"
    "v_and_b32 v83, s47, v97\n"
    "v_lshlrev_b32 v40, 16, v98\n"
    "v_and_b32 v41, s47, v98\n"
    "v_lshlrev_b32 v42, 16, v99\n"
    "v_and_b32 v43, s47, v99\n"
    "v_pk_mul_f32 v[60:61], v[80:81], v[8:9] op_sel_hi:[1,0]\n"
    "v_pk_mul_f32 v[62:63], v[82:83], v[8:9] op_sel_hi:[1,0]\n"
    "v_cvt_pk_bf16_f32 v64, v60, v61\n"
    "v_cvt_pk_bf16_f32 v65, v62, v63\n"
    "v_lshlrev_b32 v60, 16, v64\n"
    "v_and_b32 v61, s47, v64\n"
    "v_lshlrev_b32 v62, 16, v65\n"
    "v_and_b32 v63, s47, v65\n"
    "v_pk_add_f32 v[60:61], v[60:61], v[40:41]\n"
    "v_pk_add_f32 v[62:63], v[62:63], v[42:43]\n"
    "v_cvt_pk_bf16_f32 v64, v60, v61\n"
    "v_cvt_pk_bf16_f32 v65, v62, v63\n"
    "v_lshlrev_b32 v60, 16, v64\n"
    "v_and_b32 v61, s47, v64\n"
    "v_lshlrev_b32 v62, 16, v65\n"
    "v_and_b32 v63, s47, v65\n"
    "v_pk_mul_f32 v[64:65], v[60:61], v[60:61]\n"
    "v_pk_mul_f32 v[66:67], v[62:63], v[62:63]\n"
    "v_pk_fma_f32 v[64:65], v[64:65], s[62:63], v[28:29] op_sel_hi:[1,0,1]\n"
    "v_pk_fma_f32 v[66:67], v[66:67], s[62:63], v[28:29] op_sel_hi:[1,0,1]\n"
    "v_pk_mul_f32 v[64:65], v[64:65], v[60:61]\n"
    "v_pk_mul_f32 v[66:67], v[66:67], v[62:63]\n"
    "v_exp_f32 v64, v64\n"
    "v_exp_f32 v65, v65\n"
    "v_exp_f32 v66, v66\n"
    "v_exp_f32 v67, v67\n"
    "v_add_f32 v64, 1.0, v64\n"
    "v_add_f32 v65, 1.0, v65\n"
    "v_add_f32 v66, 1.0, v66\n"
    "v_add_f32 v67, 1.0, v67\n"
    "v_rcp_f32 v64, v64\n"
    "v_rcp_f32 v65, v65\n"
    "v_rcp_f32 v66, v66\n"
    "v_rcp_f32 v67, v67\n"
    "v_pk_mul_f32 v[60:61], v[60:61], v[64:65]\n"
    "v_pk_mul_f32 v[62:63], v[62:63], v[66:67]\n"
    "v_cvt_pk_bf16_f32 v64, v60, v61\n"
    "v_cvt_pk_bf16_f32 v65, v62, v63\n"
    "v_dot2_f32_bf16 v36, v64, v100, v36\n"
    "v_dot2_f32_bf16 v36, v65, v101, v36\n"
    "v_pk_mul_f32 v[68:69], v[80:81], v[8:9] op_sel:[0,1] op_sel_hi:[1,1]\n"
    "v_pk_mul_f32 v[70:71], v[82:83], v[8:9] op_sel:[0,1] op_sel_hi:[1,1]\n"
    "v_cvt_pk_bf16_f32 v72, v68, v69\n"
    "v_cvt_pk_bf16_f32 v73, v70, v71\n"
    "v_lshlrev_b32 v68, 16, v72\n"
    "v_and_b32 v69, s47, v72\n"
    "v_lshlrev_b32 v70, 16, v73\n"
    "v_and_b32 v71, s47, v73\n"
    "v_pk_add_f32 v[68:69], v[68:69], v[40:41]\n"
    "v_pk_add_f32 v[70:71], v[70:71], v[42:43]\n"
    "v_cvt_pk_bf16_f32 v72, v68, v69\n"
    "v_cvt_pk_bf16_f32 v73, v70, v71\n"
    "v_lshlrev_b32 v68, 16, v72\n"
    "v_and_b32 v69, s47, v72\n"
    "v_lshlrev_b32 v70, 16, v73\n"
    "v_and_b32 v71, s47, v73\n"
    "v_pk_mul_f32 v[72:73], v[68:69], v[68:69]\n"
    "v_pk_mul_f32 v[74:75], v[70:71], v[70:71]\n"
    "v_pk_fma_f32 v[72:73], v[72:73], s[62:63], v[28:29] op_sel_hi:[1,0,1]\n"
    "v_pk_fma_f32 v[74:75], v[74:75], s[62:63], v[28:29] op_sel_hi:[1,0,1]\n"
    "v_pk_mul_f32 v[72:73], v[72:73], v[68:69]\n"
    "v_pk_mul_f32 v[74:75], v[74:75], v[70:71]\n"
    "v_exp_f32 v72, v72\n"
    "v_exp_f32 v73, v73\n"
    "v_exp_f32 v74, v74\n"
    "v_exp_f32 v75, v75\n"
    "v_add_f32 v72, 1.0, v72\n"
    "v_add_f32 v73, 1.0, v73\n"
    "v_add_f32 v74, 1.0, v74\n"
    "v_add_f32 v75, 1.0, v75\n"
    "v_rcp_f32 v72, v72\n"
    "v_rcp_f32 v73, v73\n"
    "v_rcp_f32 v74, v74\n"
    "v_rcp_f32 v75, v75\n"
    "v_pk_mul_f32 v[68:69], v[68:69], v[72:73]\n"
    "v_pk_mul_f32 v[70:71], v[70:71], v[74:75]\n"
    "v_cvt_pk_bf16_f32 v72, v68, v69\n"
    "v_cvt_pk_bf16_f32 v73, v70, v71\n"
    "v_dot2_f32_bf16 v37, v72, v100, v37\n"
    "v_dot2_f32_bf16 v37, v73, v101, v37\n"
    "s_cmp_eq_u32 s64, 0\n"
    "s_cbranch_scc0 .LQ_GD\n"
    ".LW1:\n"
    "s_sleep 1\n"
    "s_memrealtime s[56:57]\n"
    "s_waitcnt lgkmcnt(0)\n"
    "s_sub_u32 s51, s56, s50\n"
    "s_cmp_lt_i32 s51, 0\n"
    "s_cbranch_scc1 .LW1\n"
    "global_load_dwordx4 v[52:55], v86, s[24:25] offset:-4096 sc1\n"
    "global_load_dwordx4 v[56:59], v86, s[24:25] sc1\n"
    "v_mov_b32 v96, v102\n"
    "v_mov_b32 v97, v103\n"
    "v_mov_b32 v98, v104\n"
    "v_mov_b32 v99, v105\n"
    "v_mov_b32 v100, v106\n"
    "v_mov_b32 v101, v107\n"
    "s_mov_b32 s64, 1\n"
    "s_cmp_lt_u32 s28, 3\n"
    "s_cbranch_scc1 .LQ_GP\n"
    ".LQ_GD:\n"
    "s_nop 0\n"
    "v_permlane32_swap_b32 v36, v37\n"
    "v_add_f32 v36, v36, v37\n"
    "s_lshl_b32 s30, s28, 2\n"
    "v_lshl_add_u32 v37, v91, 4, s30\n"
    "v_add_f32_dpp v36, v36, v36 row_shr:1 row_mask:0xf bank_mask:0xf bound_ctrl:1\n"
    "s_nop 1\n"
    "v_add_f32_dpp v36, v36, v36 row_shr:2 row_mask:0xf bank_mask:0xf bound_ctrl:1\n"
    "s_nop 1\n"
    "v_add_f32_dpp v36, v36, v36 row_shr:4 row_mask:0xf bank_mask:0xf bound_ctrl:1\n"
    "s_nop 1\n"
    "v_add_f32_dpp v36, v36, v36 row_shr:8 row_mask:0xf bank_mask:0xf bound_ctrl:1\n"
    "s_nop 1\n"
    "v_add_f32_dpp v36, v36, v36 row_bcast:15 row_mask:0xa bank_mask:0xf\n"
    "s_mov_b32 s58, 0x80000000\n"
    "s_mov_b32 s59, 0x80000000\n"
    "s_mov_b64 exec, s[58:59]\n"
    "ds_write_b32 v37, v36 offset:192\n"
    "s_mov_b64 exec, -1\n"
    ".LQ_GC:\n"
    "s_waitcnt vmcnt(0)\n"
    "v_xor_b32 v52, v52, v89\n"
    "v_xor_b32 v53, v53, v89\n"
    "v_xor_b32 v54, v54, v89\n"
    "v_xor_b32 v55, v55, v89\n"
    "v_xor_b32 v56, v56, v89\n"
    "v_xor_b32 v57, v57, v89\n"
    "v_xor_b32 v58, v58, v89\n"
    "v_xor_b32 v59, v59, v89\n"
    "v_or3_b32 v68, v52, v53, v54\n"
    "v_or3_b32 v69, v55, v56, v57\n"
    "v_pk_add_f32 v[52:53], v[52:53], v[56:57]\n"
    "v_or3_b32 v68, v58, v59, v68\n"
    "v_pk_add_f32 v[54:55], v[54:55], v[58:59]\n"
    "v_bitop3_b32 v68, v68, v69, s49 bitop3:0xa8\n"
    "v_cmp_ne_u32 vcc, 0, v68\n"
    "s_cmp_eq_u32 s28, 0\n"
    "v_permlane32_swap_b32 v52, v54\n"
    "v_permlane32_swap_b32 v53, v55\n"
    "s_cbranch_vccz .LQ_G2\n"
    "s_add_u32 s34, s34, 1\n"
    "s_cmp_lt_u32 s34, 0x8000\n"
    "s_cbranch_scc0 .LQ_G3\n"
    "s_sleep 2\n"
    "global_load_dwordx4 v[52:55], v86, s[24:25] offset:-4096 sc1\n"
    "global_load_dwordx4 v[56:59], v86, s[24:25] sc1\n"
    "s_branch .LQ_GC\n"
    ".LQ_G3:\n"
    "s_cmp_eq_u32 s28, 0\n"
    ".LQ_G2:\n"
    "v_pk_add_f32 v[52:53], v[52:53], v[54:55]\n"
    "v_cvt_pk_bf16_f32 v52, v52, v53\n"
    "ds_write_b32 v87, v52 offset:1536\n"
    "s_waitcnt lgkmcnt(0)\n"
    "s_cbranch_scc1 .LQ0_TAIL\n"
    "s_endpgm\n"
    ".LQ0_PREP:\n"
    "s_lshl_b32 s37, s26, 7\n"
    "s_lshl_b32 s38, s27, 2\n"
    "s_add_u32 s37, s37, s38\n"
    "v_mov_b32 v4, s37\n"
    "global_load_dword v12, v4, s[4:5]\n"
    "global_load_ushort v13, v11, s[20:21]\n"
    "v_and_b32 v38, 3, v0\n"
    "v_lshlrev_b32 v24, 2, v38\n"
    "v_lshlrev_b32 v39, 4, v3\n"
    "s_movk_i32 s30, 320\n"
    "v_mad_u32_u24 v22, v38, s30, v39\n"
    "v_mov_b32 v25, 0xff800000\n"
    "ds_write_b32 v24, v25 offset:16\n"
    "s_mul_i32 s36, s26, 81920\n"
    "s_add_u32 s36, s36, 0x11200\n"
    "s_mul_i32 s38, s27, 640\n"
    "s_add_u32 s38, s38, s36\n"
    "v_add_u32 v27, s38, v39\n"
    "s_movk_i32 s30, 20480\n"
    "v_mad_u32_u24 v27, v2, s30, v27\n"
    "s_add_u32 s61, s26, 1\n"
    "s_and_b32 s61, s61, 7\n"
    "s_lshl_b32 s42, s61, 2\n"
    "s_lshr_b32 s40, s27, 3\n"
    "s_add_u32 s42, s42, s40\n"
    "s_mul_i32 s38, s42, 20480\n"
    "s_add_u32 s38, s38, 0x11200\n"
    "s_mov_b32 s54, 0x000f000f\n"
    "s_mov_b32 s55, s54\n"
    "v_lshl_add_u32 v30, v91, 3, s38\n"
    "s_movk_i32 s30, 640\n"
    "v_mad_u32_u24 v30, v90, s30, v30\n"
    "s_lshl_b32 s38, s26, 14\n"
    "s_lshl_b32 s39, s27, 4\n"
    "s_add_u32 s38, s38, s39\n"
    "s_add_u32 s38, s38, 0xb1000\n"
    "v_lshl_add_u32 v35, v91, 3, s38\n"
    "v_lshl_add_u32 v35, v90, 9, v35\n"
    "v_lshlrev_b32 v31, 2, v90\n"
    "v_lshlrev_b32 v32, 4, v91\n"
    "v_lshl_add_u32 v33, v91, 1, s37\n"
    "v_mov_b32 v44, 0x3f803f80\n"
    "v_mov_b32 v45, v44\n"
    "v_mov_b32 v46, v44\n"
    "v_mov_b32 v47, v44\n"
    "v_mov_b32 v77, v88\n"
    "v_mov_b32 v79, v88\n"
    "s_branch .LQ_WAIT\n"
    ".p2align 12\n"
    ".LC_MAIN:\n"
    "v_lshlrev_b32 v9, 1, v3\n"
    "v_xor_b32 v9, v2, v9\n"
    "v_lshl_add_u32 v9, v9, 4, v5\n"
    "v_xor_b32 v10, 0x80, v9\n"
    "s_mov_b32 s47, 0xffff0000\n"
    "s_mov_b32 s48, 0xffffff00\n"
    "s_movk_i32 s49, 0xff\n"
    "s_add_u32 s30, s31, 0xaa00\n"
    "v_lshlrev_b32 v18, 4, v18\n"
    "v_lshl_or_b32 v18, v2, 8, v18\n"
    "v_add_u32 v18, s30, v18\n"
    "v_xor_b32 v19, 64, v18\n"
    "v_xor_b32 v20, 0x80, v18\n"
    "v_xor_b32 v21, 0xc0, v18\n"
    "v_and_b32 v38, 3, v0\n"
    "v_lshlrev_b32 v39, 4, v3\n"
    "s_movk_i32 s30, 320\n"
    "v_mad_u32_u24 v22, v38, s30, v39\n"
    "v_lshlrev_b32 v24, 2, v38\n"
    "s_lshl_b32 s36, s28, 5\n"
    "v_lshl_add_u32 v25, v3, 3, s36\n"
    "v_mad_u32_u24 v25, v2, s30, v25\n"
    "v_bfe_u32 v26, v0, 2, 3\n"
    "v_bfe_u32 v4, v0, 1, 1\n"
    "v_lshlrev_b32 v26, 1, v26\n"
    "s_lshl_b32 s36, s28, 1\n"
    "v_or_b32 v4, s36, v4\n"
    "v_xor_b32 v26, v26, v4\n"
    "v_and_b32 v4, 1, v0\n"
    "v_lshlrev_b32 v4, 3, v4\n"
    "v_lshl_or_b32 v26, v26, 4, v4\n"
    "v_bfe_u32 v4, v0, 2, 2\n"
    "v_lshl_or_b32 v26, v4, 8, v26\n"
    "v_lshl_or_b32 v26, v3, 11, v26\n"
    "v_add_u32 v26, 0x12a00, v26\n"
    "s_mul_i32 s36, s26, 81920\n"
    "s_add_u32 s36, s36, 0x11000\n"
    "s_mul_i32 s37, s27, 640\n"
    "s_lshl_b32 s38, s28, 6\n"
    "s_add_u32 s37, s37, s38\n"
    "s_add_u32 s37, s37, s36\n"
    "v_add_u32 v27, s37, v39\n"
    "s_movk_i32 s30, 20480\n"
    "v_mad_u32_u24 v27, v2, s30, v27\n"
    "s_lshr_b32 s40, s27, 3\n"
    "s_and_b32 s41, s27, 7\n"
    "s_add_u32 s61, s26, 1\n"
    "s_and_b32 s61, s61, 7\n"
    "s_lshl_b32 s42, s61, 2\n"
    "s_add_u32 s42, s42, s40\n"
    "s_lshl_b32 s37, s42, 17\n"
    "s_lshl_b32 s38, s28, 14\n"
    "s_add_u32 s37, s37, s38\n"
    "s_lshl_b32 s38, s41, 7\n"
    "s_add_u32 s37, s37, s38\n"
    "v_and_b32 v28, 7, v0\n"
    "v_and_b32 v29, 16, v0\n"
    "v_lshlrev_b32 v28, 4, v28\n"
    "v_lshlrev_b32 v29, 1, v29\n"
    "v_xad_u32 v28, v28, v29, s37\n"
    "v_lshrrev_b32 v29, 3, v1\n"
    "v_lshl_add_u32 v28, v29, 10, v28\n"
    "s_movk_i32 s30, 0x1c00\n"
    "v_xad_u32 v29, v28, 64, s30\n"
    "s_mul_i32 s37, s42, 20480\n"
    "s_add_u32 s37, s37, 0x11000\n"
    "s_mul_i32 s38, s28, 2560\n"
    "s_add_u32 s37, s37, s38\n"
    "v_and_b32 v5, 31, v0\n"
    "v_lshrrev_b32 v6, 5, v1\n"
    "v_lshl_add_u32 v30, v5, 4, s37\n"
    "s_movk_i32 s30, 640\n"
    "v_mad_u32_u24 v30, v6, s30, v30\n"
    "s_mul_i32 s38, s28, 544\n"
    "v_lshl_add_u32 v31, v5, 3, s38\n"
    "s_movk_i32 s30, 272\n"
    "v_mad_u32_u24 v31, v6, s30, v31\n"
    "s_lshl_b32 s38, s28, 4\n"
    "v_lshl_add_u32 v32, v6, 2, s38\n"
    "v_mad_u32_u24 v33, v2, s30, v39\n"
    "v_bfe_u32 v34, v0, 3, 2\n"
    "v_xor_b32 v34, s28, v34\n"
    "v_lshlrev_b32 v34, 5, v34\n"
    "v_lshl_add_u32 v34, v38, 3, v34\n"
    "v_lshl_add_u32 v34, v4, 7, v34\n"
    "v_lshl_add_u32 v34, v3, 10, v34\n"
    "v_add_u32 v34, 0x6a00, v34\n"
    "s_lshl_b32 s37, s41, 5\n"
    "s_add_u32 s37, s37, s42\n"
    "s_lshl_b32 s37, s37, 9\n"
    "s_lshl_b32 s38, s28, 7\n"
    "s_add_u32 s37, s37, s38\n"
    "s_add_u32 s37, s37, 0xb1000\n"
    "v_lshl_add_u32 v35, v2, 3, s37\n"
    "v_mov_b32 v38, 0\n"
    "s_mov_b32 s50, 0x3db504f3\n"
    "s_mov_b32 s52, 0x3fb8aa3b\n"
    "s_mov_b32 s54, 0x000f000f\n"
    "s_mov_b32 s55, 0x000f000f\n"
    "s_waitcnt vmcnt(2)\n"
    "global_load_lds_dwordx4 v8, s[6:7] offset:3072 nt\n"
    "s_add_u32 m0, s31, 0x12a00\n"
    "s_waitcnt vmcnt(2)\n"
    "global_load_lds_dwordx4 v9, s[8:9] nt\n"
    "s_waitcnt vmcnt(2)\n"
    "global_load_lds_dwordx4 v9, s[8:9] offset:1024 nt\n"
    "s_waitcnt vmcnt(2)\n"
    "global_load_lds_dwordx4 v10, s[8:9] offset:2048 nt\n"
    "ds_read_b128 v[40:43], v18\n"
    "ds_read_b128 v[44:47], v19\n"
    "ds_read_b128 v[48:51], v20\n"
    "ds_read_b128 v[52:55], v21\n"
    "s_waitcnt vmcnt(3)\n"
    "global_load_lds_dwordx4 v10, s[8:9] offset:3072 nt\n"
    "s_lshl_b32 s30, s28, 11\n"
    "s_add_u32 m0, s30, 0x6a00\n"
    "s_barrier\n"
    "ds_read_b128 v[56:59], v22 offset:1536\n"
    "ds_read_b128 v[60:63], v22 offset:1600\n"
    "ds_read_b128 v[64:67], v22 offset:1664\n"
    "ds_read_b128 v[68:71], v22 offset:1728\n"
    "ds_read_b32 v72, v38 offset:8\n"
    "global_load_lds_dwordx4 v28, s[12:13] nt\n"
    "s_waitcnt lgkmcnt(4)\n"
    "v_mfma_f32_16x16x32_bf16 v[76:79], v[40:43], v[56:59], 0\n"
    "s_waitcnt lgkmcnt(3)\n"
    "v_mfma_f32_16x16x32_bf16 v[76:79], v[44:47], v[60:63], v[76:79]\n"
    "s_waitcnt lgkmcnt(2)\n"
    "v_mfma_f32_16x16x32_bf16 v[76:79], v[48:51], v[64:67], v[76:79]\n"
    "s_waitcnt lgkmcnt(1)\n"
    "v_mfma_f32_16x16x32_bf16 v[76:79], v[52:55], v[68:71], v[76:79]\n"
    "s_nop 7\n"
    "v_cvt_pk_bf16_f32 v40, v76, v77\n"
    "v_cvt_pk_bf16_f32 v41, v78, v79\n"
    "v_lshlrev_b32 v76, 16, v40\n"
    "v_and_b32 v77, s47, v40\n"
    "v_lshlrev_b32 v78, 16, v41\n"
    "v_and_b32 v79, s47, v41\n"
    "v_pk_mul_f32 v[76:77], v[76:77], s[50:51] op_sel_hi:[1,0]\n"
    "v_pk_mul_f32 v[78:79], v[78:79], s[50:51] op_sel_hi:[1,0]\n"
    "v_cvt_pk_bf16_f32 v40, v76, v77\n"
    "v_cvt_pk_bf16_f32 v41, v78, v79\n"
    "v_lshlrev_b32 v76, 16, v40\n"
    "v_and_b32 v77, s47, v40\n"
    "v_lshlrev_b32 v78, 16, v41\n"
    "v_and_b32 v79, s47, v41\n"
    "v_pk_mul_f32 v[76:77], v[76:77], s[52:53] op_sel_hi:[1,0]\n"
    "v_pk_mul_f32 v[78:79], v[78:79], s[52:53] op_sel_hi:[1,0]\n"
    "s_mov_b64 exec, s[54:55]\n"
    "v_max_f32 v40, v76, v77\n"
    "v_max3_f32 v40, v78, v79, v40\n"
    "ds_max_f32 v24, v40 offset:16\n"
    "s_mov_b64 exec, -1\n"
    "s_waitcnt lgkmcnt(0)\n"
    "s_barrier\n"
    "ds_read_b32 v40, v24 offset:16\n"
    "v_and_b32 v73, s49, v72\n"
    "s_waitcnt lgkmcnt(0)\n"
    "v_sub_f32 v76, v76, v40\n"
    "v_sub_f32 v77, v77, v40\n"
    "v_sub_f32 v78, v78, v40\n"
    "v_sub_f32 v79, v79, v40\n"
    "v_exp_f32 v76, v76\n"
    "v_exp_f32 v77, v77\n"
    "v_exp_f32 v78, v78\n"
    "v_exp_f32 v79, v79\n"
    "v_cvt_pk_bf16_f32 v40, v76, v77\n"
    "v_cvt_pk_bf16_f32 v41, v78, v79\n"
    "s_mov_b64 exec, s[54:55]\n"
    "ds_write_b64 v25, v[40:41] offset:2816\n"
    "s_mov_b64 exec, -1\n"
    "s_waitcnt lgkmcnt(0)\n"
    "s_barrier\n"
    "ds_read_b128 v[56:59], v22 offset:2816\n"
    "ds_read_b128 v[60:63], v22 offset:2880\n"
    "ds_read_b128 v[64:67], v22 offset:2944\n"
    "ds_read_b128 v[68:71], v22 offset:3008\n"
    "s_waitcnt vmcnt(1)\n"
    "global_load_lds_dwordx4 v29, s[12:13] offset:1024 nt\n"
    "s_barrier\n"
    "ds_read_b64_tr_b16 v[40:41], v26 offset:0\n"
    "ds_read_b64_tr_b16 v[42:43], v26 offset:1024\n"
    "ds_read_b64_tr_b16 v[44:45], v26 offset:8192\n"
    "ds_read_b64_tr_b16 v[46:47], v26 offset:9216\n"
    "ds_read_b64_tr_b16 v[48:49], v26 offset:16384\n"
    "ds_read_b64_tr_b16 v[50:51], v26 offset:17408\n"
    "ds_read_b64_tr_b16 v[52:53], v26 offset:24576\n"
    "ds_read_b64_tr_b16 v[54:55], v26 offset:25600\n"
    "s_waitcnt lgkmcnt(6)\n"
    "v_mfma_f32_16x16x32_bf16 v[76:79], v[40:43], v[56:59], 0\n"
    "s_waitcnt lgkmcnt(4)\n"
    "v_mfma_f32_16x16x32_bf16 v[76:79], v[44:47], v[60:63], v[76:79]\n"
    "s_waitcnt lgkmcnt(2)\n"
    "v_mfma_f32_16x16x32_bf16 v[76:79], v[48:51], v[64:67], v[76:79]\n"
    "s_waitcnt lgkmcnt(0)\n"
    "v_mfma_f32_16x16x32_bf16 v[76:79], v[52:55], v[68:71], v[76:79]\n"
    "s_mov_b64 exec, s[54:55]\n"
    "s_nop 6\n"
    "v_and_or_b32 v76, v76, s48, v73\n"
    "v_and_or_b32 v77, v77, s48, v73\n"
    "v_and_or_b32 v78, v78, s48, v73\n"
    "v_and_or_b32 v79, v79, s48, v73\n"
    "global_store_dwordx4 v27, v[76:79], s[24:25] sc1\n"
    "s_mov_b64 exec, -1\n"
    "s_mov_b32 s34, 0\n"
    "s_barrier\n"
    "global_load_dwordx4 v[40:43], v30, s[24:25] sc1\n"
    "global_load_dwordx4 v[44:47], v30, s[24:25] offset:1280 sc1\n"
    "s_waitcnt vmcnt(2)\n"
    "s_barrier\n"
    "ds_read2_b32 v[74:75], v32 offset0:64 offset1:66\n"
    "ds_read_b64_tr_b16 v[56:57], v34 offset:0\n"
    "ds_read_b64_tr_b16 v[58:59], v34 offset:512\n"
    "ds_read_b64_tr_b16 v[60:61], v34 offset:4096\n"
    "ds_read_b64_tr_b16 v[62:63], v34 offset:4608\n"
    "ds_read_b64_tr_b16 v[64:65], v34 offset:8192\n"
    "ds_read_b64_tr_b16 v[66:67], v34 offset:8704\n"
    "ds_read_b64_tr_b16 v[68:69], v34 offset:12288\n"
    "ds_read_b64_tr_b16 v[70:71], v34 offset:12800\n"
    ".LC_CHK:\n"
    "s_waitcnt vmcnt(0) lgkmcnt(0)\n"
    "v_xor_b32 v40, v40, v73\n"
    "v_xor_b32 v41, v41, v73\n"
    "v_xor_b32 v42, v42, v73\n"
    "v_xor_b32 v43, v43, v73\n"
    "v_xor_b32 v44, v44, v73\n"
    "v_xor_b32 v45, v45, v73\n"
    "v_xor_b32 v46, v46, v73\n"
    "v_xor_b32 v47, v47, v73\n"
    "v_or3_b32 v48, v40, v41, v42\n"
    "v_or3_b32 v49, v43, v44, v45\n"
    "v_pk_mul_f32 v[40:41], v[40:41], v[74:75] op_sel_hi:[1,0]\n"
    "v_or3_b32 v48, v46, v47, v48\n"
    "v_pk_mul_f32 v[42:43], v[42:43], v[74:75] op_sel_hi:[1,0]\n"
    "v_bitop3_b32 v48, v48, v49, s49 bitop3:0xa8\n"
    "v_pk_fma_f32 v[40:41], v[44:45], v[74:75], v[40:41] op_sel:[0,1,0] op_sel_hi:[1,1,1]\n"
    "v_cmp_ne_u32 vcc, 0, v48\n"
    "v_pk_fma_f32 v[42:43], v[46:47], v[74:75], v[42:43] op_sel:[0,1,0] op_sel_hi:[1,1,1]\n"
    "v_cvt_pk_bf16_f32 v40, v40, v41\n"
    "v_cvt_pk_bf16_f32 v41, v42, v43\n"
    "s_cbranch_vccz .LC_OK\n"
    "s_add_u32 s34, s34, 1\n"
    "s_cmp_lt_u32 s34, 0x8000\n"
    "s_cbranch_scc0 .LC_OK\n"
    "s_sleep 2\n"
    "global_load_dwordx4 v[40:43], v30, s[24:25] sc1\n"
    "global_load_dwordx4 v[44:47], v30, s[24:25] offset:1280 sc1\n"
    "s_branch .LC_CHK\n"
    ".LC_OK:\n"
    "ds_write_b64 v31, v[40:41] offset:4096\n"
    "s_cmp_lt_u32 s28, 4\n"
    "s_waitcnt lgkmcnt(0)\n"
    "s_barrier\n"
    "s_cbranch_scc0 .LC_END\n"
    "ds_read_b128 v[40:43], v33 offset:4096\n"
    "ds_read_b128 v[44:47], v33 offset:4160\n"
    "ds_read_b128 v[48:51], v33 offset:4224\n"
    "ds_read_b128 v[52:55], v33 offset:4288\n"
    "s_waitcnt lgkmcnt(3)\n"
    "v_mfma_f32_16x16x32_bf16 v[76:79], v[40:43], v[56:59], 0\n"
    "s_waitcnt lgkmcnt(2)\n"
    "v_mfma_f32_16x16x32_bf16 v[76:79], v[44:47], v[60:63], v[76:79]\n"
    "s_waitcnt lgkmcnt(1)\n"
    "v_mfma_f32_16x16x32_bf16 v[76:79], v[48:51], v[64:67], v[76:79]\n"
    "s_waitcnt lgkmcnt(0)\n"
    "v_mfma_f32_16x16x32_bf16 v[76:79], v[52:55], v[68:71], v[76:79]\n"
    "s_nop 7\n"
    "v_pk_add_f32 v[76:77], v[76:77], v[78:79]\n"
    "v_add_f32 v76, v76, v77\n"
    "v_mov_b32 v77, v76\n"
    "s_nop 1\n"
    "v_permlane16_swap_b32 v76, v77\n"
    "v_add_f32 v76, v76, v77\n"
    "v_mov_b32 v77, v76\n"
    "s_nop 1\n"
    "v_permlane32_swap_b32 v76, v77\n"
    "v_add_f32 v76, v76, v77\n"
    "v_mov_b32 v77, v72\n"
    "s_mov_b64 exec, 0xffff\n"
    "global_store_dwordx2 v35, v[76:77], s[24:25] sc1\n"
    ".LC_END:\n"
    "s_endpgm\n"
    ".LQ0_TAIL:\n"
    "s_barrier\n"
    "ds_read_b128 v[52:55], v32 offset:192\n"
    "s_waitcnt lgkmcnt(0)\n"
    "v_lshlrev_b32 v13, 16, v13\n"
    "v_lshlrev_b32 v14, 16, v12\n"
    "v_and_b32 v15, s47, v12\n"
    "s_mov_b32 s64, 0\n"
    "s_mov_b32 s65, -1\n"
    "v_cndmask_b32 v12, v14, v15, s[64:65]\n"
    "v_pk_add_f32 v[52:53], v[52:53], v[54:55]\n"
    "v_add_f32 v52, v52, v53\n"
    "v_cvt_pk_bf16_f32 v52, v52, v52\n"
    "v_and_b32 v52, s47, v52\n"
    "v_add_f32 v52, v52, v13\n"
    "v_cvt_pk_bf16_f32 v52, v52, v52\n"
    "v_and_b32 v52, s47, v52\n"
    "v_mul_f32 v53, 0xbfb8aa3b, v52\n"
    "v_exp_f32 v53, v53\n"
    "s_nop 0\n"
    "v_add_f32 v53, 1.0, v53\n"
    "v_rcp_f32 v53, v53\n"
    "s_nop 0\n"
    "v_mul_f32 v52, v52, v53\n"
    "v_cvt_pk_bf16_f32 v52, v52, v52\n"
    "v_and_b32 v14, s47, v52\n"
    "s_barrier\n"
    "ds_read_b32 v76, v24 offset:16\n"
    "s_barrier\n"
    "ds_read_b128 v[52:55], v22 offset:2816\n"
    "ds_read_b128 v[56:59], v22 offset:2880\n"
    "ds_read_b128 v[60:63], v22 offset:2944\n"
    "ds_read_b128 v[64:67], v22 offset:3008\n"
    "s_waitcnt lgkmcnt(3)\n"
    "v_mfma_f32_16x16x32_bf16 v[68:71], v[44:47], v[52:55], 0\n"
    "s_waitcnt lgkmcnt(2)\n"
    "v_mfma_f32_16x16x32_bf16 v[68:71], v[44:47], v[56:59], v[68:71]\n"
    "s_waitcnt lgkmcnt(1)\n"
    "v_mfma_f32_16x16x32_bf16 v[68:71], v[44:47], v[60:63], v[68:71]\n"
    "s_waitcnt lgkmcnt(0)\n"
    "v_mfma_f32_16x16x32_bf16 v[68:71], v[44:47], v[64:67], v[68:71]\n"
    "s_mov_b64 exec, s[54:55]\n"
    "s_nop 6\n"
    "v_mov_b32 v78, v68\n"
    "global_store_dwordx4 v27, v[76:79], s[24:25] sc1\n"
    "s_mov_b64 exec, -1\n"
    "s_barrier\n"
    "s_sleep 15\n"
    "s_mov_b32 s34, 0\n"
    "global_load_dwordx2 v[80:81], v30, s[24:25] sc1\n"
    "s_sleep 5\n"
    "global_load_dwordx2 v[82:83], v30, s[24:25] sc1\n"
    "s_sleep 5\n"
    ".LML_L:\n"
    "global_load_dwordx2 v[84:85], v30, s[24:25] sc1\n"
    "s_waitcnt vmcnt(2)\n"
    "v_cmp_ne_u32 vcc, v81, v88\n"
    "v_mov_b32 v52, v80\n"
    "s_cbranch_vccz .LML_OK\n"
    "global_load_dwordx2 v[80:81], v30, s[24:25] sc1\n"
    "s_waitcnt vmcnt(2)\n"
    "v_cmp_ne_u32 vcc, v83, v88\n"
    "v_mov_b32 v52, v82\n"
    "s_cbranch_vccz .LML_OK\n"
    "global_load_dwordx2 v[82:83], v30, s[24:25] sc1\n"
    "s_waitcnt vmcnt(2)\n"
    "v_cmp_ne_u32 vcc, v85, v88\n"
    "v_mov_b32 v52, v84\n"
    "s_cbranch_vccz .LML_OK\n"
    "s_add_u32 s34, s34, 3\n"
    "s_cmp_lt_u32 s34, 0x8000\n"
    "s_cbranch_scc1 .LML_L\n"
    ".LML_OK:\n"
    "s_barrier\n"
    "v_mov_b32 v54, v52\n"
    "s_nop 1\n"
    "v_permlane32_swap_b32 v52, v54\n"
    "s_nop 1\n"
    "v_max_f32_dpp v55, v52, v52 quad_perm:[1,0,3,2] row_mask:0xf bank_mask:0xf\n"
    "s_nop 1\n"
    "v_max_f32_dpp v55, v55, v55 quad_perm:[2,3,0,1] row_mask:0xf bank_mask:0xf\n"
    "s_nop 1\n"
    "v_max_f32_dpp v55, v55, v55 row_half_mirror row_mask:0xf bank_mask:0xf\n"
    "s_nop 1\n"
    "v_max_f32_dpp v55, v55, v55 row_mirror row_mask:0xf bank_mask:0xf\n"
    "v_mov_b32 v56, v55\n"
    "s_nop 1\n"
    "v_permlane16_swap_b32 v55, v56\n"
    "v_max_f32 v55, v55, v56\n"
    "v_sub_f32 v52, v52, v55\n"
    "v_exp_f32 v52, v52\n"
    "s_nop 0\n"
    "v_mul_f32 v54, v52, v54\n"
    "s_nop 1\n"
    "v_add_f32_dpp v54, v54, v54 quad_perm:[1,0,3,2] row_mask:0xf bank_mask:0xf\n"
    "s_nop 1\n"
    "v_add_f32_dpp v54, v54, v54 quad_perm:[2,3,0,1] row_mask:0xf bank_mask:0xf\n"
    "s_nop 1\n"
    "v_add_f32_dpp v54, v54, v54 row_half_mirror row_mask:0xf bank_mask:0xf\n"
    "s_nop 1\n"
    "v_add_f32_dpp v54, v54, v54 row_mirror row_mask:0xf bank_mask:0xf\n"
    "v_mov_b32 v56, v54\n"
    "s_nop 1\n"
    "v_permlane16_swap_b32 v54, v56\n"
    "v_add_f32 v54, v54, v56\n"
    "v_rcp_f32 v54, v54\n"
    "s_nop 0\n"
    "v_mul_f32 v52, v52, v54\n"
    "ds_write_b32 v31, v52 offset:256\n"
    "s_waitcnt lgkmcnt(0)\n"
    "s_barrier\n"
    "s_barrier\n"
    "s_sleep 14\n"
    "s_mov_b32 s34, 0\n"
    "global_load_dwordx2 v[64:65], v35, s[24:25] sc1\n"
    "s_sleep 8\n"
    ".LF_L:\n"
    "global_load_dwordx2 v[66:67], v35, s[24:25] sc1\n"
    "s_waitcnt vmcnt(1)\n"
    "v_cmp_ne_u32 vcc, v65, v88\n"
    "v_mov_b32 v52, v64\n"
    "s_cbranch_vccz .LF_OK\n"
    "global_load_dwordx2 v[64:65], v35, s[24:25] sc1\n"
    "s_waitcnt vmcnt(1)\n"
    "v_cmp_ne_u32 vcc, v67, v88\n"
    "v_mov_b32 v52, v66\n"
    "s_cbranch_vccz .LF_OK\n"
    "s_add_u32 s34, s34, 2\n"
    "s_cmp_lt_u32 s34, 0x8000\n"
    "s_cbranch_scc1 .LF_L\n"
    ".LF_OK:\n"
    "s_cmp_eq_u32 s2, 0\n"
    "s_cbranch_scc0 .LF_NE\n"
    "s_mov_b64 exec, 1\n"
    "global_store_dword v11, v88, s[24:25] sc1\n"
    "s_mov_b64 exec, -1\n"
    ".LF_NE:\n"
    "v_add_f32_dpp v52, v52, v52 row_shr:1 row_mask:0xf bank_mask:0xf bound_ctrl:1\n"
    "s_nop 1\n"
    "v_add_f32_dpp v52, v52, v52 row_shr:2 row_mask:0xf bank_mask:0xf bound_ctrl:1\n"
    "s_nop 1\n"
    "v_add_f32_dpp v52, v52, v52 row_shr:4 row_mask:0xf bank_mask:0xf bound_ctrl:1\n"
    "s_nop 1\n"
    "v_add_f32_dpp v52, v52, v52 row_shr:8 row_mask:0xf bank_mask:0xf bound_ctrl:1\n"
    "s_nop 1\n"
    "v_add_f32_dpp v52, v52, v52 row_bcast:15 row_mask:0xa bank_mask:0xf\n"
    "v_cvt_pk_bf16_f32 v52, v52, v52\n"
    "v_and_b32 v52, s47, v52\n"
    "v_mul_f32 v52, v52, v14\n"
    "v_cvt_pk_bf16_f32 v52, v52, v52\n"
    "v_and_b32 v52, s47, v52\n"
    "v_add_f32 v52, v52, v12\n"
    "v_cvt_pk_bf16_f32 v52, v52, v52\n"
    "s_mov_b64 exec, s[58:59]\n"
    "global_store_short v33, v52, s[22:23]\n"
    ".LF_END:\n"
    "s_endpgm\n"
    :: "{s[0:1]}"(__builtin_amdgcn_kernarg_segment_ptr()), "{s2}"(__builtin_amdgcn_workgroup_id_x()), "{v0}"(__builtin_amdgcn_workitem_id_x())
    : "v1", "v2", "v3", "v4", "v5", "v6", "v7", "v8", "v9", "v10", "v11", "v12", "v13", "v14", "v15", "v16", "v17", "v18", "v19", "v20", "v21", "v22", "v23", "v24", "v25", "v26", "v27", "v28", "v29", "v30", "v31", "v32", "v33", "v34", "v35", "v36", "v37", "v38", "v39", "v40", "v41", "v42", "v43", "v44", "v45", "v46", "v47", "v48", "v49", "v50", "v51", "v52", "v53", "v54", "v55", "v56", "v57", "v58", "v59", "v60", "v61", "v62", "v63", "v64", "v65", "v66", "v67", "v68", "v69", "v70", "v71", "v72", "v73", "v74", "v75", "v76", "v77", "v78", "v79", "v80", "v81", "v82", "v83", "v84", "v85", "v86", "v87", "v88", "v89", "v90", "v91", "v92", "v93", "v94", "v95", "v96", "v97", "v98", "v99", "v100", "v101", "v102", "v103", "v104", "v105", "v106", "v107", "s3", "s4", "s5", "s6", "s7", "s8", "s9", "s10", "s11", "s12", "s13", "s14", "s15", "s16", "s17", "s18", "s19", "s20", "s21", "s22", "s23", "s24", "s25", "s26", "s27", "s28", "s29", "s30", "s31", "s34", "s35", "s36", "s37", "s38", "s39", "s40", "s41", "s42", "s43", "s44", "s45", "s46", "s47", "s48", "s49", "s50", "s51", "s52", "s53", "s54", "s55", "s56", "s57", "s58", "s59", "s60", "s61", "s62", "s63", "s64", "s65", "s66", "s67", "s68", "s69", "s70", "s71", "s72", "s73", "s74", "s75", "s76", "s77", "s78", "s79", "s80", "s81", "s82", "s83", "s84", "s85", "s86", "s87", "s88", "s89", "s90", "s91", "s92", "s93", "s94", "s95", "vcc", "scc", "memory");
}

extern "C" void kernel_launch(void* const* d_in, const int* in_sizes, int n_in, void* d_out, int out_size, void* d_ws, size_t ws_size, hipStream_t stream) {
  (void)hipFuncSetAttribute((const void*)fused_decode, hipFuncAttributeMaxDynamicSharedMemorySize, 109056);
  fused_decode<<<256, 768, 109056, stream>>>((const u16*)d_in[0], (const u16*)d_in[1], (const u16*)d_in[2], (const u16*)d_in[3], (const u16*)d_in[4],
      (const u16*)d_in[6], (const u16*)d_in[7], (const u16*)d_in[8], (const u16*)d_in[9], (u16*)d_out, (char*)d_ws);
}
```
